```python
import torch

def _softmax(x, axis=-1):
    return torch.softmax(x, dim=axis)

def _gelu(x):
    return torch.nn.functional.gelu(x, approximate='tanh')

def _silu(x):
    return torch.nn.functional.silu(x)

def _sigmoid(x):
    return torch.sigmoid(x)

def _layernorm(x, axis=-1, eps=1e-05):
    m = x.mean(dim=axis, keepdim=True)
    v = x.var(dim=axis, keepdim=True, unbiased=False)
    return (x - m) / torch.sqrt(v + eps)

def _scatter_add(tgt, idx, src, axis=0):
    return tgt.clone().index_add_(axis, idx, src)

def _segment_sum(src, seg, n):
    out = torch.zeros((n,) + src.shape[1:], dtype=src.dtype)
    return out.index_add_(0, seg, src)

def _pad(x, pw):
    flat = tuple((v for pair in reversed(pw) for v in pair))
    return torch.nn.functional.pad(x, flat)
kernel_name = 'adaLN_cond__ssm_conv_h3__glu_gated_geglu__prompt'

def setup_inputs():

    _w = lambda a: (lambda t: t.to(torch.bfloat16) if t.is_floating_point() else t)(torch.as_tensor(a))

    def _A_setup():
        B, S, D = (1, 2048, 1024)
        ks = 4
        return {'q': _w((lambda *s: torch.randn(*s, dtype=torch.bfloat16) * 0.05)(B, S, D)), 'k': _w((lambda *s: torch.randn(*s, dtype=torch.bfloat16) * 1.6)(B, S, D)), 'v': _w((lambda *s: torch.randn(*s, dtype=torch.bfloat16) * 1.6)(B, S, D)), 'shift_k': _w((lambda *s: torch.randn(*s, dtype=torch.bfloat16) * 1.6)(D, ks) * 0.5), 'ssm_k': _w((lambda *s: torch.randn(*s, dtype=torch.bfloat16) * 1.6)(D, S) * (1.0 / S ** 0.5)), 'ssm_D': _w((lambda *s: torch.randn(*s, dtype=torch.bfloat16) * 1.6)(D))}
    _A_in = _A_setup()

    def _B_setup():
        B, S, Dm, Df = (1, 2048, 1024, 3584)
        return {'x': _w((lambda *s: torch.randn(*s, dtype=torch.bfloat16) * 0.05)(B, S, Dm)), 'w_gate': _w((lambda *s: torch.randn(*s, dtype=torch.bfloat16) * 3.2)(Dm, Df) * Dm ** (-0.5)), 'w_up': _w((lambda *s: torch.randn(*s, dtype=torch.bfloat16) * 3.2)(Dm, Df) * Dm ** (-0.5)), 'w_down': _w((lambda *s: torch.randn(*s, dtype=torch.bfloat16) * 3.2)(Df, Dm) * Df ** (-0.5))}
    _B_in = _B_setup()
    return {'A_q': _A_in['q'], 'A_k': _A_in['k'], 'A_v': _A_in['v'], 'A_shift_k': _A_in['shift_k'], 'A_ssm_k': _A_in['ssm_k'], 'A_ssm_D': _A_in['ssm_D'], 'B_x': _B_in['x'], 'B_w_gate': _B_in['w_gate'], 'B_w_up': _B_in['w_up'], 'B_w_down': _B_in['w_down'], 'c': _w((lambda *s: torch.randn(*s, dtype=torch.bfloat16) * 0.1)(1, 1024) * 8.0), 'w_mod': _w((lambda *s: torch.randn(*s, dtype=torch.bfloat16) * 0.1)(1024, 6 * 1024) * 1024 ** (-0.5) * 8.0)}

def reference(A_q, A_k, A_v, A_shift_k, A_ssm_k, A_ssm_D, B_x, B_w_gate, B_w_up, B_w_down, c, w_mod):

    def _A_ref(q, k, v, shift_k, ssm_k, ssm_D):
        B, S, D = q.shape
        ks = shift_k.shape[-1]
        k_pad = _pad(k, ((0, 0), (ks - 1, 0), (0, 0)))
        k_shift = torch.zeros_like(k)
        for t in range(ks):
            k_shift = k_shift + k_pad[:, t:t + S, :] * shift_k[:, ks - 1 - t][None, None, :]
        kv = k_shift * v
        L = 2 * S
        kv_pad = _pad(kv, ((0, 0), (0, S), (0, 0)))
        kf_pad = _pad(ssm_k, ((0, 0), (0, S)))
        kv_f = (lambda x, axes=(-2, -1): torch.fft.rfft2(x.float(), dim=axes))(kv_pad[:, None, :, :], axes=(1, 2))
        kf_f = (lambda x, axes=(-2, -1): torch.fft.rfft2(x.float(), dim=axes))(kf_pad[:, None, :], axes=(1, 2))
        kf_f = (lambda x, *d: x.permute(*d))(kf_f, 1, 2, 0)
        y_f = kv_f * kf_f[None, :, :, :]
        y = (lambda x, s, axes=(-2, -1): torch.fft.irfft2(x, s=s, dim=axes).to(torch.bfloat16))(y_f, (1, L), axes=(1, 2))[:, 0, :S, :]
        y = y + kv * ssm_D[None, None, :]
        return q * y

    def _B_ref(x, w_gate, w_up, w_down):
        g = torch.einsum('bsd,df->bsf', x, w_gate)
        u = torch.einsum('bsd,df->bsf', x, w_up)
        h = _gelu(g) * u
        return torch.einsum('bsf,fd->bsd', h, w_down)
    x = A_q
    mod = (_silu(c) @ w_mod).reshape(c.shape[0], 6, 1, -1)
    sa, ca, ga, sb, cb, gb = (mod[:, 0].clone(), mod[:, 1].clone(), mod[:, 2].clone(), mod[:, 3].clone(), mod[:, 4].clone(), mod[:, 5].clone())
    h = _layernorm(x, axis=-1) * (1 + ca) + sa
    x = x + ga * _A_ref(h, A_k, A_v, A_shift_k, A_ssm_k, A_ssm_D)
    h = _layernorm(x, axis=-1) * (1 + cb) + sb
    x = x + gb * _B_ref(h, B_w_gate, B_w_up, B_w_down)
    return x
```

```cpp
#include <hip/hip_runtime.h>
#include <hip/hip_bf16.h>
#include <stdint.h>
typedef unsigned short u16;
__device__ __forceinline__ float bf2f(u16 b){ return __uint_as_float(((unsigned)b)<<16); }
__device__ __forceinline__ u16 f2bf(float f){ unsigned u=__float_as_uint(f); u += 0x7FFFu + ((u>>16)&1u); return (u16)(u>>16); }
__device__ __forceinline__ float rb(float f){ return bf2f(f2bf(f)); }
constexpr int S=2048, D=1024, DF=3584;

__global__ void k_mod(const u16* c, const u16* wmod, float* mod){
  int j = blockIdx.x*blockDim.x+threadIdx.x; if(j>=6*D) return;
  float acc=0.f;
  for(int k=0;k<D;k++){ float x=bf2f(c[k]); float s=rb(x/(1.f+expf(-x))); acc += s*bf2f(wmod[(size_t)k*6*D+j]); }
  mod[j]=rb(acc);
}
__global__ void k_kv(const u16* k, const u16* v, const u16* sk, u16* kv){
  int i = blockIdx.x*blockDim.x+threadIdx.x; int s=i/D, d=i%D;
  float ks=0.f;
  for(int t=0;t<4;t++){ int ss=s-3+t; float kk = ss>=0? bf2f(k[(size_t)ss*D+d]):0.f; ks = rb(ks + rb(kk*bf2f(sk[d*4+3-t]))); }
  kv[i]=f2bf(ks*bf2f(v[i]));
}
__global__ void k_conv(const u16* kv, const u16* h, const u16* Dv, float* y){
  int i = blockIdx.x*blockDim.x+threadIdx.x; int s=i/D, d=i%D;
  float acc=0.f;
  for(int t=0;t<=s;t++) acc += bf2f(kv[(size_t)t*D+d])*bf2f(h[(size_t)d*S + s-t]);
  float yy=rb(acc);
  y[i]=rb(yy + rb(bf2f(kv[i])*bf2f(Dv[d])));
}
__global__ void k_mid(const u16* q, const float* y, const float* mod, u16* x1, u16* h2){
  __shared__ float red[256]; __shared__ float xs[1024];
  int s=blockIdx.x, t=threadIdx.x;
  float v[4]; float sum=0;
  for(int j=0;j<4;j++){ v[j]=bf2f(q[(size_t)s*D+t+256*j]); sum+=v[j]; }
  red[t]=sum; __syncthreads(); for(int o=128;o>0;o>>=1){ if(t<o) red[t]+=red[t+o]; __syncthreads(); }
  float m=rb(red[0]/D); __syncthreads();
  float m32=red[0]/D;
  __syncthreads();
  float sq=0; for(int j=0;j<4;j++){ float dd=v[j]-m32; sq+=dd*dd; }
  red[t]=sq; __syncthreads(); for(int o=128;o>0;o>>=1){ if(t<o) red[t]+=red[t+o]; __syncthreads(); }
  float var=rb(red[0]/D); __syncthreads();
  float den=rb(sqrtf(rb(var+1e-5f)));
  sum=0;
  for(int j=0;j<4;j++){ int d=t+256*j; float n=rb(rb(v[j]-m)/den); float h=rb(rb(n*rb(1.f+mod[D+d]))+mod[d]);
    float a=rb(h*y[(size_t)s*D+d]); float x=rb(v[j]+rb(mod[2*D+d]*a)); v[j]=x; x1[(size_t)s*D+d]=f2bf(x); sum+=x; }
  red[t]=sum; __syncthreads(); for(int o=128;o>0;o>>=1){ if(t<o) red[t]+=red[t+o]; __syncthreads(); }
  m=rb(red[0]/D); m32=red[0]/D; __syncthreads();
  sq=0; for(int j=0;j<4;j++){ float dd=v[j]-m32; sq+=dd*dd; }
  red[t]=sq; __syncthreads(); for(int o=128;o>0;o>>=1){ if(t<o) red[t]+=red[t+o]; __syncthreads(); }
  var=rb(red[0]/D); __syncthreads();
  den=rb(sqrtf(rb(var+1e-5f)));
  for(int j=0;j<4;j++){ int d=t+256*j; float n=rb(rb(v[j]-m)/den); float h=rb(rb(n*rb(1.f+mod[4*D+d]))+mod[3*D+d]); h2[(size_t)s*D+d]=f2bf(h); }
}
__global__ void k_g1(const u16* h2, const u16* wg, const u16* wu, u16* act){
  int f = blockIdx.x*blockDim.x+threadIdx.x; int s=blockIdx.y;
  float g=0,u=0;
  for(int k=0;k<D;k++){ float a=bf2f(h2[(size_t)s*D+k]); g+=a*bf2f(wg[(size_t)k*DF+f]); u+=a*bf2f(wu[(size_t)k*DF+f]); }
  g=rb(g); u=rb(u);
  float ge=rb(0.5f*g*(1.f+tanhf(0.7978845608028654f*(g+0.044715f*g*g*g))));
  act[(size_t)s*DF+f]=f2bf(ge*u);
}
__global__ void k_g2(const u16* act, const u16* wd, const u16* x1, const float* mod, u16* out){
  int d = blockIdx.x*blockDim.x+threadIdx.x; int s=blockIdx.y;
  float a=0;
  for(int k=0;k<DF;k++) a+=bf2f(act[(size_t)s*DF+k])*bf2f(wd[(size_t)k*D+d]);
  a=rb(a);
  out[(size_t)s*D+d]=f2bf(bf2f(x1[(size_t)s*D+d]) + rb(mod[5*D+d]*a));
}
extern "C" void kernel_launch(void* const* d_in, const int* in_sizes, int n_in,
                              void* d_out, int out_size, void* d_ws, size_t ws_size, hipStream_t stream) {
  const u16* q=(const u16*)d_in[0]; const u16* k=(const u16*)d_in[1]; const u16* v=(const u16*)d_in[2];
  const u16* sk=(const u16*)d_in[3]; const u16* hk=(const u16*)d_in[4]; const u16* Dv=(const u16*)d_in[5];
  const u16* wg=(const u16*)d_in[7]; const u16* wu=(const u16*)d_in[8]; const u16* wd=(const u16*)d_in[9];
  const u16* c=(const u16*)d_in[10]; const u16* wmod=(const u16*)d_in[11];
  char* ws=(char*)d_ws;
  float* mod=(float*)ws; u16* kv=(u16*)(ws+(1<<20)); float* y=(float*)(ws+(8<<20)); u16* x1=(u16*)(ws+(20<<20)); u16* h2=(u16*)(ws+(28<<20)); u16* act=(u16*)(ws+(36<<20));
  k_mod<<<6*D/256,256,0,stream>>>(c,wmod,mod);
  k_kv<<<S*D/256,256,0,stream>>>(k,v,sk,kv);
  k_conv<<<S*D/256,256,0,stream>>>(kv,hk,Dv,y);
  k_mid<<<S,256,0,stream>>>(q,y,mod,x1,h2);
  k_g1<<<dim3(DF/256,S),256,0,stream>>>(h2,wg,wu,act);
  k_g2<<<dim3(D/256,S),256,0,stream>>>(act,wd,x1,mod,(u16*)d_out);
}
```

```cpp
#include <hip/hip_runtime.h>
#include <stdint.h>
#define G1_ASM \
  "s_add_u32 m0, s48, 0\n" \
  "s_nop 0\n" \
  "buffer_load_dwordx4 v240, s[36:39], s49 offen offset:0 lds\n" \
  "buffer_load_dwordx4 a[0:3], v244, s[40:43], s50 offen offset:0 sc1\n" \
  "buffer_load_dwordx4 v241, s[36:39], s49 offen offset:1024 lds\n" \
  "buffer_load_dwordx4 a[4:7], v244, s[40:43], s50 offen offset:1024 sc1\n" \
  "buffer_load_dwordx4 v242, s[36:39], s49 offen offset:2048 lds\n" \
  "buffer_load_dwordx4 a[8:11], v245, s[40:43], s50 offen offset:0 sc1\n" \
  "buffer_load_dwordx4 v243, s[36:39], s49 offen offset:3072 lds\n" \
  "buffer_load_dwordx4 a[12:15], v245, s[40:43], s50 offen offset:1024 sc1\n" \
  "s_add_u32 s49, s49, 229376\n" \
  "s_add_u32 s50, s50, 4096\n" \
  "v_mov_b32 v0, 0\n" \
  "v_mov_b32 v1, 0\n" \
  "v_mov_b32 v2, 0\n" \
  "v_mov_b32 v3, 0\n" \
  "v_mov_b32 v4, 0\n" \
  "v_mov_b32 v5, 0\n" \
  "v_mov_b32 v6, 0\n" \
  "v_mov_b32 v7, 0\n" \
  "v_mov_b32 v8, 0\n" \
  "v_mov_b32 v9, 0\n" \
  "v_mov_b32 v10, 0\n" \
  "v_mov_b32 v11, 0\n" \
  "v_mov_b32 v12, 0\n" \
  "v_mov_b32 v13, 0\n" \
  "v_mov_b32 v14, 0\n" \
  "v_mov_b32 v15, 0\n" \
  "v_mov_b32 v16, 0\n" \
  "v_mov_b32 v17, 0\n" \
  "v_mov_b32 v18, 0\n" \
  "v_mov_b32 v19, 0\n" \
  "v_mov_b32 v20, 0\n" \
  "v_mov_b32 v21, 0\n" \
  "v_mov_b32 v22, 0\n" \
  "v_mov_b32 v23, 0\n" \
  "v_mov_b32 v24, 0\n" \
  "v_mov_b32 v25, 0\n" \
  "v_mov_b32 v26, 0\n" \
  "v_mov_b32 v27, 0\n" \
  "v_mov_b32 v28, 0\n" \
  "v_mov_b32 v29, 0\n" \
  "v_mov_b32 v30, 0\n" \
  "v_mov_b32 v31, 0\n" \
  "v_mov_b32 v32, 0\n" \
  "v_mov_b32 v33, 0\n" \
  "v_mov_b32 v34, 0\n" \
  "v_mov_b32 v35, 0\n" \
  "v_mov_b32 v36, 0\n" \
  "v_mov_b32 v37, 0\n" \
  "v_mov_b32 v38, 0\n" \
  "v_mov_b32 v39, 0\n" \
  "v_mov_b32 v40, 0\n" \
  "v_mov_b32 v41, 0\n" \
  "v_mov_b32 v42, 0\n" \
  "v_mov_b32 v43, 0\n" \
  "v_mov_b32 v44, 0\n" \
  "v_mov_b32 v45, 0\n" \
  "v_mov_b32 v46, 0\n" \
  "v_mov_b32 v47, 0\n" \
  "v_mov_b32 v48, 0\n" \
  "v_mov_b32 v49, 0\n" \
  "v_mov_b32 v50, 0\n" \
  "v_mov_b32 v51, 0\n" \
  "v_mov_b32 v52, 0\n" \
  "v_mov_b32 v53, 0\n" \
  "v_mov_b32 v54, 0\n" \
  "v_mov_b32 v55, 0\n" \
  "v_mov_b32 v56, 0\n" \
  "v_mov_b32 v57, 0\n" \
  "v_mov_b32 v58, 0\n" \
  "v_mov_b32 v59, 0\n" \
  "v_mov_b32 v60, 0\n" \
  "v_mov_b32 v61, 0\n" \
  "v_mov_b32 v62, 0\n" \
  "v_mov_b32 v63, 0\n" \
  "v_mov_b32 v64, 0\n" \
  "v_mov_b32 v65, 0\n" \
  "v_mov_b32 v66, 0\n" \
  "v_mov_b32 v67, 0\n" \
  "v_mov_b32 v68, 0\n" \
  "v_mov_b32 v69, 0\n" \
  "v_mov_b32 v70, 0\n" \
  "v_mov_b32 v71, 0\n" \
  "v_mov_b32 v72, 0\n" \
  "v_mov_b32 v73, 0\n" \
  "v_mov_b32 v74, 0\n" \
  "v_mov_b32 v75, 0\n" \
  "v_mov_b32 v76, 0\n" \
  "v_mov_b32 v77, 0\n" \
  "v_mov_b32 v78, 0\n" \
  "v_mov_b32 v79, 0\n" \
  "v_mov_b32 v80, 0\n" \
  "v_mov_b32 v81, 0\n" \
  "v_mov_b32 v82, 0\n" \
  "v_mov_b32 v83, 0\n" \
  "v_mov_b32 v84, 0\n" \
  "v_mov_b32 v85, 0\n" \
  "v_mov_b32 v86, 0\n" \
  "v_mov_b32 v87, 0\n" \
  "v_mov_b32 v88, 0\n" \
  "v_mov_b32 v89, 0\n" \
  "v_mov_b32 v90, 0\n" \
  "v_mov_b32 v91, 0\n" \
  "v_mov_b32 v92, 0\n" \
  "v_mov_b32 v93, 0\n" \
  "v_mov_b32 v94, 0\n" \
  "v_mov_b32 v95, 0\n" \
  "v_mov_b32 v96, 0\n" \
  "v_mov_b32 v97, 0\n" \
  "v_mov_b32 v98, 0\n" \
  "v_mov_b32 v99, 0\n" \
  "v_mov_b32 v100, 0\n" \
  "v_mov_b32 v101, 0\n" \
  "v_mov_b32 v102, 0\n" \
  "v_mov_b32 v103, 0\n" \
  "v_mov_b32 v104, 0\n" \
  "v_mov_b32 v105, 0\n" \
  "v_mov_b32 v106, 0\n" \
  "v_mov_b32 v107, 0\n" \
  "v_mov_b32 v108, 0\n" \
  "v_mov_b32 v109, 0\n" \
  "v_mov_b32 v110, 0\n" \
  "v_mov_b32 v111, 0\n" \
  "v_mov_b32 v112, 0\n" \
  "v_mov_b32 v113, 0\n" \
  "v_mov_b32 v114, 0\n" \
  "v_mov_b32 v115, 0\n" \
  "v_mov_b32 v116, 0\n" \
  "v_mov_b32 v117, 0\n" \
  "v_mov_b32 v118, 0\n" \
  "v_mov_b32 v119, 0\n" \
  "v_mov_b32 v120, 0\n" \
  "v_mov_b32 v121, 0\n" \
  "v_mov_b32 v122, 0\n" \
  "v_mov_b32 v123, 0\n" \
  "v_mov_b32 v124, 0\n" \
  "v_mov_b32 v125, 0\n" \
  "v_mov_b32 v126, 0\n" \
  "v_mov_b32 v127, 0\n" \
  "v_mov_b32 v128, 0\n" \
  "v_mov_b32 v129, 0\n" \
  "v_mov_b32 v130, 0\n" \
  "v_mov_b32 v131, 0\n" \
  "v_mov_b32 v132, 0\n" \
  "v_mov_b32 v133, 0\n" \
  "v_mov_b32 v134, 0\n" \
  "v_mov_b32 v135, 0\n" \
  "v_mov_b32 v136, 0\n" \
  "v_mov_b32 v137, 0\n" \
  "v_mov_b32 v138, 0\n" \
  "v_mov_b32 v139, 0\n" \
  "v_mov_b32 v140, 0\n" \
  "v_mov_b32 v141, 0\n" \
  "v_mov_b32 v142, 0\n" \
  "v_mov_b32 v143, 0\n" \
  "v_mov_b32 v144, 0\n" \
  "v_mov_b32 v145, 0\n" \
  "v_mov_b32 v146, 0\n" \
  "v_mov_b32 v147, 0\n" \
  "v_mov_b32 v148, 0\n" \
  "v_mov_b32 v149, 0\n" \
  "v_mov_b32 v150, 0\n" \
  "v_mov_b32 v151, 0\n" \
  "v_mov_b32 v152, 0\n" \
  "v_mov_b32 v153, 0\n" \
  "v_mov_b32 v154, 0\n" \
  "v_mov_b32 v155, 0\n" \
  "v_mov_b32 v156, 0\n" \
  "v_mov_b32 v157, 0\n" \
  "v_mov_b32 v158, 0\n" \
  "v_mov_b32 v159, 0\n" \
  "v_mov_b32 v160, 0\n" \
  "v_mov_b32 v161, 0\n" \
  "v_mov_b32 v162, 0\n" \
  "v_mov_b32 v163, 0\n" \
  "v_mov_b32 v164, 0\n" \
  "v_mov_b32 v165, 0\n" \
  "v_mov_b32 v166, 0\n" \
  "v_mov_b32 v167, 0\n" \
  "v_mov_b32 v168, 0\n" \
  "v_mov_b32 v169, 0\n" \
  "v_mov_b32 v170, 0\n" \
  "v_mov_b32 v171, 0\n" \
  "v_mov_b32 v172, 0\n" \
  "v_mov_b32 v173, 0\n" \
  "v_mov_b32 v174, 0\n" \
  "v_mov_b32 v175, 0\n" \
  "v_mov_b32 v176, 0\n" \
  "v_mov_b32 v177, 0\n" \
  "v_mov_b32 v178, 0\n" \
  "v_mov_b32 v179, 0\n" \
  "v_mov_b32 v180, 0\n" \
  "v_mov_b32 v181, 0\n" \
  "v_mov_b32 v182, 0\n" \
  "v_mov_b32 v183, 0\n" \
  "v_mov_b32 v184, 0\n" \
  "v_mov_b32 v185, 0\n" \
  "v_mov_b32 v186, 0\n" \
  "v_mov_b32 v187, 0\n" \
  "v_mov_b32 v188, 0\n" \
  "v_mov_b32 v189, 0\n" \
  "v_mov_b32 v190, 0\n" \
  "v_mov_b32 v191, 0\n" \
  "v_mov_b32 v192, 0\n" \
  "v_mov_b32 v193, 0\n" \
  "v_mov_b32 v194, 0\n" \
  "v_mov_b32 v195, 0\n" \
  "v_mov_b32 v196, 0\n" \
  "v_mov_b32 v197, 0\n" \
  "v_mov_b32 v198, 0\n" \
  "v_mov_b32 v199, 0\n" \
  "v_mov_b32 v200, 0\n" \
  "v_mov_b32 v201, 0\n" \
  "v_mov_b32 v202, 0\n" \
  "v_mov_b32 v203, 0\n" \
  "v_mov_b32 v204, 0\n" \
  "v_mov_b32 v205, 0\n" \
  "v_mov_b32 v206, 0\n" \
  "v_mov_b32 v207, 0\n" \
  "v_mov_b32 v208, 0\n" \
  "v_mov_b32 v209, 0\n" \
  "v_mov_b32 v210, 0\n" \
  "v_mov_b32 v211, 0\n" \
  "v_mov_b32 v212, 0\n" \
  "v_mov_b32 v213, 0\n" \
  "v_mov_b32 v214, 0\n" \
  "v_mov_b32 v215, 0\n" \
  "v_mov_b32 v216, 0\n" \
  "v_mov_b32 v217, 0\n" \
  "v_mov_b32 v218, 0\n" \
  "v_mov_b32 v219, 0\n" \
  "v_mov_b32 v220, 0\n" \
  "v_mov_b32 v221, 0\n" \
  "v_mov_b32 v222, 0\n" \
  "v_mov_b32 v223, 0\n" \
  "s_add_u32 m0, s48, 16384\n" \
  "s_nop 0\n" \
  "buffer_load_dwordx4 v240, s[36:39], s49 offen offset:0 lds\n" \
  "buffer_load_dwordx4 a[16:19], v244, s[40:43], s50 offen offset:0 sc1\n" \
  "buffer_load_dwordx4 v241, s[36:39], s49 offen offset:1024 lds\n" \
  "buffer_load_dwordx4 a[20:23], v244, s[40:43], s50 offen offset:1024 sc1\n" \
  "buffer_load_dwordx4 v242, s[36:39], s49 offen offset:2048 lds\n" \
  "buffer_load_dwordx4 a[24:27], v245, s[40:43], s50 offen offset:0 sc1\n" \
  "buffer_load_dwordx4 v243, s[36:39], s49 offen offset:3072 lds\n" \
  "buffer_load_dwordx4 a[28:31], v245, s[40:43], s50 offen offset:1024 sc1\n" \
  "s_add_u32 s49, s49, 229376\n" \
  "s_add_u32 s50, s50, 4096\n" \
  "v_and_b32 v248, 0x70, v246\n" \
  "v_lshlrev_b32 v248, 3, v248\n" \
  "s_mov_b32 s51, 0\n" \
  "s_waitcnt vmcnt(8)\n" \
  "s_barrier\n" \
  "ds_read_b64_tr_b16 a[128:129], v224 offset:0\n" \
  "ds_read_b64_tr_b16 a[130:131], v224 offset:4096\n" \
  "ds_read_b64_tr_b16 a[132:133], v225 offset:0\n" \
  "ds_read_b64_tr_b16 a[134:135], v225 offset:4096\n" \
  "ds_read_b64_tr_b16 a[136:137], v224 offset:8192\n" \
  "ds_read_b64_tr_b16 a[138:139], v224 offset:12288\n" \
  "ds_read_b64_tr_b16 a[176:177], v230 offset:0\n" \
  "ds_read_b64_tr_b16 a[178:179], v230 offset:4096\n" \
  "L_G1_loop0_%=:\n" \
  "s_waitcnt lgkmcnt(6)\n" \
  "v_mfma_f32_16x16x32_bf16 v[0:3], a[128:131], a[0:3], v[0:3]\n" \
  "s_add_u32 m0, s48, 32768\n" \
  "ds_read_b64_tr_b16 a[140:141], v225 offset:8192\n" \
  "v_mfma_f32_16x16x32_bf16 v[4:7], a[128:131], a[4:7], v[4:7]\n" \
  "ds_read_b64_tr_b16 a[142:143], v225 offset:12288\n" \
  "v_mfma_f32_16x16x32_bf16 v[8:11], a[128:131], a[8:11], v[8:11]\n" \
  "buffer_load_dwordx4 v240, s[36:39], s49 offen offset:0 lds\n" \
  "v_mfma_f32_16x16x32_bf16 v[12:15], a[128:131], a[12:15], v[12:15]\n" \
  "s_waitcnt lgkmcnt(6)\n" \
  "v_mfma_f32_16x16x32_bf16 v[16:19], a[132:135], a[0:3], v[16:19]\n" \
  "ds_read_b64_tr_b16 a[144:145], v226 offset:0\n" \
  "v_mfma_f32_16x16x32_bf16 v[20:23], a[132:135], a[4:7], v[20:23]\n" \
  "ds_read_b64_tr_b16 a[146:147], v226 offset:4096\n" \
  "v_mfma_f32_16x16x32_bf16 v[24:27], a[132:135], a[8:11], v[24:27]\n" \
  "buffer_load_dwordx4 a[32:35], v244, s[40:43], s50 offen offset:0 sc1\n" \
  "v_mfma_f32_16x16x32_bf16 v[28:31], a[132:135], a[12:15], v[28:31]\n" \
  "s_waitcnt lgkmcnt(6)\n" \
  "v_mfma_f32_16x16x32_bf16 v[32:35], a[136:139], a[0:3], v[32:35]\n" \
  "ds_read_b64_tr_b16 a[148:149], v227 offset:0\n" \
  "v_mfma_f32_16x16x32_bf16 v[36:39], a[136:139], a[4:7], v[36:39]\n" \
  "ds_read_b64_tr_b16 a[150:151], v227 offset:4096\n" \
  "v_mfma_f32_16x16x32_bf16 v[40:43], a[136:139], a[8:11], v[40:43]\n" \
  "buffer_load_dwordx4 v241, s[36:39], s49 offen offset:1024 lds\n" \
  "v_mfma_f32_16x16x32_bf16 v[44:47], a[136:139], a[12:15], v[44:47]\n" \
  "s_waitcnt lgkmcnt(6)\n" \
  "v_mfma_f32_16x16x32_bf16 v[192:195], a[176:179], a[0:3], v[192:195]\n" \
  "ds_read_b64_tr_b16 a[180:181], v231 offset:0\n" \
  "v_mfma_f32_16x16x32_bf16 v[196:199], a[176:179], a[4:7], v[196:199]\n" \
  "ds_read_b64_tr_b16 a[182:183], v231 offset:4096\n" \
  "s_waitcnt lgkmcnt(6)\n" \
  "v_mfma_f32_16x16x32_bf16 v[48:51], a[140:143], a[0:3], v[48:51]\n" \
  "ds_read_b64_tr_b16 a[152:153], v226 offset:8192\n" \
  "buffer_load_dwordx4 a[36:39], v244, s[40:43], s50 offen offset:1024 sc1\n" \
  "v_mfma_f32_16x16x32_bf16 v[52:55], a[140:143], a[4:7], v[52:55]\n" \
  "ds_read_b64_tr_b16 a[154:155], v226 offset:12288\n" \
  "v_mfma_f32_16x16x32_bf16 v[56:59], a[140:143], a[8:11], v[56:59]\n" \
  "v_mfma_f32_16x16x32_bf16 v[60:63], a[140:143], a[12:15], v[60:63]\n" \
  "s_waitcnt lgkmcnt(6)\n" \
  "v_mfma_f32_16x16x32_bf16 v[64:67], a[144:147], a[0:3], v[64:67]\n" \
  "ds_read_b64_tr_b16 a[156:157], v227 offset:8192\n" \
  "buffer_load_dwordx4 v242, s[36:39], s49 offen offset:2048 lds\n" \
  "v_mfma_f32_16x16x32_bf16 v[68:71], a[144:147], a[4:7], v[68:71]\n" \
  "ds_read_b64_tr_b16 a[158:159], v227 offset:12288\n" \
  "v_mfma_f32_16x16x32_bf16 v[72:75], a[144:147], a[8:11], v[72:75]\n" \
  "v_mfma_f32_16x16x32_bf16 v[76:79], a[144:147], a[12:15], v[76:79]\n" \
  "s_waitcnt lgkmcnt(6)\n" \
  "v_mfma_f32_16x16x32_bf16 v[80:83], a[148:151], a[0:3], v[80:83]\n" \
  "ds_read_b64_tr_b16 a[160:161], v228 offset:0\n" \
  "buffer_load_dwordx4 a[40:43], v245, s[40:43], s50 offen offset:0 sc1\n" \
  "v_mfma_f32_16x16x32_bf16 v[84:87], a[148:151], a[4:7], v[84:87]\n" \
  "ds_read_b64_tr_b16 a[162:163], v228 offset:4096\n" \
  "v_mfma_f32_16x16x32_bf16 v[88:91], a[148:151], a[8:11], v[88:91]\n" \
  "v_mfma_f32_16x16x32_bf16 v[92:95], a[148:151], a[12:15], v[92:95]\n" \
  "s_waitcnt lgkmcnt(6)\n" \
  "v_mfma_f32_16x16x32_bf16 v[200:203], a[180:183], a[0:3], v[200:203]\n" \
  "ds_read_b64_tr_b16 a[184:185], v230 offset:8192\n" \
  "buffer_load_dwordx4 v243, s[36:39], s49 offen offset:3072 lds\n" \
  "v_mfma_f32_16x16x32_bf16 v[204:207], a[180:183], a[4:7], v[204:207]\n" \
  "ds_read_b64_tr_b16 a[186:187], v230 offset:12288\n" \
  "s_waitcnt lgkmcnt(6)\n" \
  "v_mfma_f32_16x16x32_bf16 v[96:99], a[152:155], a[0:3], v[96:99]\n" \
  "ds_read_b64_tr_b16 a[164:165], v229 offset:0\n" \
  "v_mfma_f32_16x16x32_bf16 v[100:103], a[152:155], a[4:7], v[100:103]\n" \
  "ds_read_b64_tr_b16 a[166:167], v229 offset:4096\n" \
  "v_mfma_f32_16x16x32_bf16 v[104:107], a[152:155], a[8:11], v[104:107]\n" \
  "buffer_load_dwordx4 a[44:47], v245, s[40:43], s50 offen offset:1024 sc1\n" \
  "v_mfma_f32_16x16x32_bf16 v[108:111], a[152:155], a[12:15], v[108:111]\n" \
  "s_add_u32 s49, s49, 229376\n" \
  "s_waitcnt lgkmcnt(6)\n" \
  "v_mfma_f32_16x16x32_bf16 v[112:115], a[156:159], a[0:3], v[112:115]\n" \
  "ds_read_b64_tr_b16 a[168:169], v228 offset:8192\n" \
  "s_add_u32 s50, s50, 4096\n" \
  "v_mfma_f32_16x16x32_bf16 v[116:119], a[156:159], a[4:7], v[116:119]\n" \
  "ds_read_b64_tr_b16 a[170:171], v228 offset:12288\n" \
  "v_mfma_f32_16x16x32_bf16 v[120:123], a[156:159], a[8:11], v[120:123]\n" \
  "v_mfma_f32_16x16x32_bf16 v[124:127], a[156:159], a[12:15], v[124:127]\n" \
  "s_waitcnt lgkmcnt(6)\n" \
  "v_mfma_f32_16x16x32_bf16 v[128:131], a[160:163], a[0:3], v[128:131]\n" \
  "ds_read_b64_tr_b16 a[172:173], v229 offset:8192\n" \
  "v_mfma_f32_16x16x32_bf16 v[132:135], a[160:163], a[4:7], v[132:135]\n" \
  "ds_read_b64_tr_b16 a[174:175], v229 offset:12288\n" \
  "v_mfma_f32_16x16x32_bf16 v[136:139], a[160:163], a[8:11], v[136:139]\n" \
  "v_mfma_f32_16x16x32_bf16 v[140:143], a[160:163], a[12:15], v[140:143]\n" \
  "s_waitcnt lgkmcnt(6)\n" \
  "v_mfma_f32_16x16x32_bf16 v[208:211], a[184:187], a[0:3], v[208:211]\n" \
  "ds_read_b64_tr_b16 a[188:189], v231 offset:8192\n" \
  "v_mfma_f32_16x16x32_bf16 v[212:215], a[184:187], a[4:7], v[212:215]\n" \
  "ds_read_b64_tr_b16 a[190:191], v231 offset:12288\n" \
  "s_waitcnt vmcnt(8)\n" \
  "s_barrier\n" \
  "s_waitcnt lgkmcnt(6)\n" \
  "v_mfma_f32_16x16x32_bf16 v[144:147], a[164:167], a[0:3], v[144:147]\n" \
  "ds_read_b64_tr_b16 a[128:129], v224 offset:16384\n" \
  "v_mfma_f32_16x16x32_bf16 v[148:151], a[164:167], a[4:7], v[148:151]\n" \
  "ds_read_b64_tr_b16 a[130:131], v224 offset:20480\n" \
  "v_mfma_f32_16x16x32_bf16 v[152:155], a[164:167], a[8:11], v[152:155]\n" \
  "v_mfma_f32_16x16x32_bf16 v[156:159], a[164:167], a[12:15], v[156:159]\n" \
  "s_waitcnt lgkmcnt(6)\n" \
  "v_mfma_f32_16x16x32_bf16 v[160:163], a[168:171], a[0:3], v[160:163]\n" \
  "ds_read_b64_tr_b16 a[132:133], v225 offset:16384\n" \
  "buffer_load_dword v247, v248, s[60:63], s79 offen\n" \
  "v_mfma_f32_16x16x32_bf16 v[164:167], a[168:171], a[4:7], v[164:167]\n" \
  "ds_read_b64_tr_b16 a[134:135], v225 offset:20480\n" \
  "v_mfma_f32_16x16x32_bf16 v[168:171], a[168:171], a[8:11], v[168:171]\n" \
  "s_add_u32 s79, s79, 458752\n" \
  "v_mfma_f32_16x16x32_bf16 v[172:175], a[168:171], a[12:15], v[172:175]\n" \
  "s_waitcnt lgkmcnt(6)\n" \
  "v_mfma_f32_16x16x32_bf16 v[176:179], a[172:175], a[0:3], v[176:179]\n" \
  "ds_read_b64_tr_b16 a[136:137], v224 offset:24576\n" \
  "v_mfma_f32_16x16x32_bf16 v[180:183], a[172:175], a[4:7], v[180:183]\n" \
  "ds_read_b64_tr_b16 a[138:139], v224 offset:28672\n" \
  "v_mfma_f32_16x16x32_bf16 v[184:187], a[172:175], a[8:11], v[184:187]\n" \
  "v_mfma_f32_16x16x32_bf16 v[188:191], a[172:175], a[12:15], v[188:191]\n" \
  "s_waitcnt lgkmcnt(6)\n" \
  "v_mfma_f32_16x16x32_bf16 v[216:219], a[188:191], a[0:3], v[216:219]\n" \
  "ds_read_b64_tr_b16 a[176:177], v230 offset:16384\n" \
  "v_mfma_f32_16x16x32_bf16 v[220:223], a[188:191], a[4:7], v[220:223]\n" \
  "ds_read_b64_tr_b16 a[178:179], v230 offset:20480\n" \
  "s_waitcnt lgkmcnt(6)\n" \
  "v_mfma_f32_16x16x32_bf16 v[0:3], a[128:131], a[16:19], v[0:3]\n" \
  "s_add_u32 m0, s48, 49152\n" \
  "ds_read_b64_tr_b16 a[140:141], v225 offset:24576\n" \
  "v_mfma_f32_16x16x32_bf16 v[4:7], a[128:131], a[20:23], v[4:7]\n" \
  "ds_read_b64_tr_b16 a[142:143], v225 offset:28672\n" \
  "v_mfma_f32_16x16x32_bf16 v[8:11], a[128:131], a[24:27], v[8:11]\n" \
  "buffer_load_dwordx4 v240, s[36:39], s49 offen offset:0 lds\n" \
  "v_mfma_f32_16x16x32_bf16 v[12:15], a[128:131], a[28:31], v[12:15]\n" \
  "s_waitcnt lgkmcnt(6)\n" \
  "v_mfma_f32_16x16x32_bf16 v[16:19], a[132:135], a[16:19], v[16:19]\n" \
  "ds_read_b64_tr_b16 a[144:145], v226 offset:16384\n" \
  "v_mfma_f32_16x16x32_bf16 v[20:23], a[132:135], a[20:23], v[20:23]\n" \
  "ds_read_b64_tr_b16 a[146:147], v226 offset:20480\n" \
  "v_mfma_f32_16x16x32_bf16 v[24:27], a[132:135], a[24:27], v[24:27]\n" \
  "buffer_load_dwordx4 a[48:51], v244, s[40:43], s50 offen offset:0 sc1\n" \
  "v_mfma_f32_16x16x32_bf16 v[28:31], a[132:135], a[28:31], v[28:31]\n" \
  "s_waitcnt lgkmcnt(6)\n" \
  "v_mfma_f32_16x16x32_bf16 v[32:35], a[136:139], a[16:19], v[32:35]\n" \
  "ds_read_b64_tr_b16 a[148:149], v227 offset:16384\n" \
  "v_mfma_f32_16x16x32_bf16 v[36:39], a[136:139], a[20:23], v[36:39]\n" \
  "ds_read_b64_tr_b16 a[150:151], v227 offset:20480\n" \
  "v_mfma_f32_16x16x32_bf16 v[40:43], a[136:139], a[24:27], v[40:43]\n" \
  "buffer_load_dwordx4 v241, s[36:39], s49 offen offset:1024 lds\n" \
  "v_mfma_f32_16x16x32_bf16 v[44:47], a[136:139], a[28:31], v[44:47]\n" \
  "s_waitcnt lgkmcnt(6)\n" \
  "v_mfma_f32_16x16x32_bf16 v[192:195], a[176:179], a[16:19], v[192:195]\n" \
  "ds_read_b64_tr_b16 a[180:181], v231 offset:16384\n" \
  "v_mfma_f32_16x16x32_bf16 v[196:199], a[176:179], a[20:23], v[196:199]\n" \
  "ds_read_b64_tr_b16 a[182:183], v231 offset:20480\n" \
  "s_waitcnt lgkmcnt(6)\n" \
  "v_mfma_f32_16x16x32_bf16 v[48:51], a[140:143], a[16:19], v[48:51]\n" \
  "ds_read_b64_tr_b16 a[152:153], v226 offset:24576\n" \
  "buffer_load_dwordx4 a[52:55], v244, s[40:43], s50 offen offset:1024 sc1\n" \
  "v_mfma_f32_16x16x32_bf16 v[52:55], a[140:143], a[20:23], v[52:55]\n" \
  "ds_read_b64_tr_b16 a[154:155], v226 offset:28672\n" \
  "v_mfma_f32_16x16x32_bf16 v[56:59], a[140:143], a[24:27], v[56:59]\n" \
  "v_mfma_f32_16x16x32_bf16 v[60:63], a[140:143], a[28:31], v[60:63]\n" \
  "s_waitcnt lgkmcnt(6)\n" \
  "v_mfma_f32_16x16x32_bf16 v[64:67], a[144:147], a[16:19], v[64:67]\n" \
  "ds_read_b64_tr_b16 a[156:157], v227 offset:24576\n" \
  "buffer_load_dwordx4 v242, s[36:39], s49 offen offset:2048 lds\n" \
  "v_mfma_f32_16x16x32_bf16 v[68:71], a[144:147], a[20:23], v[68:71]\n" \
  "ds_read_b64_tr_b16 a[158:159], v227 offset:28672\n" \
  "v_mfma_f32_16x16x32_bf16 v[72:75], a[144:147], a[24:27], v[72:75]\n" \
  "v_mfma_f32_16x16x32_bf16 v[76:79], a[144:147], a[28:31], v[76:79]\n" \
  "s_waitcnt lgkmcnt(6)\n" \
  "v_mfma_f32_16x16x32_bf16 v[80:83], a[148:151], a[16:19], v[80:83]\n" \
  "ds_read_b64_tr_b16 a[160:161], v228 offset:16384\n" \
  "buffer_load_dwordx4 a[56:59], v245, s[40:43], s50 offen offset:0 sc1\n" \
  "v_mfma_f32_16x16x32_bf16 v[84:87], a[148:151], a[20:23], v[84:87]\n" \
  "ds_read_b64_tr_b16 a[162:163], v228 offset:20480\n" \
  "v_mfma_f32_16x16x32_bf16 v[88:91], a[148:151], a[24:27], v[88:91]\n" \
  "v_mfma_f32_16x16x32_bf16 v[92:95], a[148:151], a[28:31], v[92:95]\n" \
  "s_waitcnt lgkmcnt(6)\n" \
  "v_mfma_f32_16x16x32_bf16 v[200:203], a[180:183], a[16:19], v[200:203]\n" \
  "ds_read_b64_tr_b16 a[184:185], v230 offset:24576\n" \
  "buffer_load_dwordx4 v243, s[36:39], s49 offen offset:3072 lds\n" \
  "v_mfma_f32_16x16x32_bf16 v[204:207], a[180:183], a[20:23], v[204:207]\n" \
  "ds_read_b64_tr_b16 a[186:187], v230 offset:28672\n" \
  "s_waitcnt lgkmcnt(6)\n" \
  "v_mfma_f32_16x16x32_bf16 v[96:99], a[152:155], a[16:19], v[96:99]\n" \
  "ds_read_b64_tr_b16 a[164:165], v229 offset:16384\n" \
  "v_mfma_f32_16x16x32_bf16 v[100:103], a[152:155], a[20:23], v[100:103]\n" \
  "ds_read_b64_tr_b16 a[166:167], v229 offset:20480\n" \
  "v_mfma_f32_16x16x32_bf16 v[104:107], a[152:155], a[24:27], v[104:107]\n" \
  "buffer_load_dwordx4 a[60:63], v245, s[40:43], s50 offen offset:1024 sc1\n" \
  "v_mfma_f32_16x16x32_bf16 v[108:111], a[152:155], a[28:31], v[108:111]\n" \
  "s_add_u32 s49, s49, 229376\n" \
  "s_waitcnt lgkmcnt(6)\n" \
  "v_mfma_f32_16x16x32_bf16 v[112:115], a[156:159], a[16:19], v[112:115]\n" \
  "ds_read_b64_tr_b16 a[168:169], v228 offset:24576\n" \
  "s_add_u32 s50, s50, 4096\n" \
  "v_mfma_f32_16x16x32_bf16 v[116:119], a[156:159], a[20:23], v[116:119]\n" \
  "ds_read_b64_tr_b16 a[170:171], v228 offset:28672\n" \
  "v_mfma_f32_16x16x32_bf16 v[120:123], a[156:159], a[24:27], v[120:123]\n" \
  "v_mfma_f32_16x16x32_bf16 v[124:127], a[156:159], a[28:31], v[124:127]\n" \
  "s_waitcnt lgkmcnt(6)\n" \
  "v_mfma_f32_16x16x32_bf16 v[128:131], a[160:163], a[16:19], v[128:131]\n" \
  "ds_read_b64_tr_b16 a[172:173], v229 offset:24576\n" \
  "v_mfma_f32_16x16x32_bf16 v[132:135], a[160:163], a[20:23], v[132:135]\n" \
  "ds_read_b64_tr_b16 a[174:175], v229 offset:28672\n" \
  "v_mfma_f32_16x16x32_bf16 v[136:139], a[160:163], a[24:27], v[136:139]\n" \
  "v_mfma_f32_16x16x32_bf16 v[140:143], a[160:163], a[28:31], v[140:143]\n" \
  "s_waitcnt lgkmcnt(6)\n" \
  "v_mfma_f32_16x16x32_bf16 v[208:211], a[184:187], a[16:19], v[208:211]\n" \
  "ds_read_b64_tr_b16 a[188:189], v231 offset:24576\n" \
  "v_mfma_f32_16x16x32_bf16 v[212:215], a[184:187], a[20:23], v[212:215]\n" \
  "ds_read_b64_tr_b16 a[190:191], v231 offset:28672\n" \
  "s_waitcnt vmcnt(9)\n" \
  "s_barrier\n" \
  "s_waitcnt lgkmcnt(6)\n" \
  "v_mfma_f32_16x16x32_bf16 v[144:147], a[164:167], a[16:19], v[144:147]\n" \
  "ds_read_b64_tr_b16 a[128:129], v224 offset:32768\n" \
  "v_mfma_f32_16x16x32_bf16 v[148:151], a[164:167], a[20:23], v[148:151]\n" \
  "ds_read_b64_tr_b16 a[130:131], v224 offset:36864\n" \
  "buffer_load_dword v247, v248, s[80:83], s78 offen\n" \
  "v_mfma_f32_16x16x32_bf16 v[152:155], a[164:167], a[24:27], v[152:155]\n" \
  "v_mfma_f32_16x16x32_bf16 v[156:159], a[164:167], a[28:31], v[156:159]\n" \
  "s_add_u32 s78, s78, 1024\n" \
  "s_waitcnt lgkmcnt(6)\n" \
  "v_mfma_f32_16x16x32_bf16 v[160:163], a[168:171], a[16:19], v[160:163]\n" \
  "ds_read_b64_tr_b16 a[132:133], v225 offset:32768\n" \
  "v_mfma_f32_16x16x32_bf16 v[164:167], a[168:171], a[20:23], v[164:167]\n" \
  "ds_read_b64_tr_b16 a[134:135], v225 offset:36864\n" \
  "v_mfma_f32_16x16x32_bf16 v[168:171], a[168:171], a[24:27], v[168:171]\n" \
  "v_mfma_f32_16x16x32_bf16 v[172:175], a[168:171], a[28:31], v[172:175]\n" \
  "s_waitcnt lgkmcnt(6)\n" \
  "v_mfma_f32_16x16x32_bf16 v[176:179], a[172:175], a[16:19], v[176:179]\n" \
  "ds_read_b64_tr_b16 a[136:137], v224 offset:40960\n" \
  "v_mfma_f32_16x16x32_bf16 v[180:183], a[172:175], a[20:23], v[180:183]\n" \
  "ds_read_b64_tr_b16 a[138:139], v224 offset:45056\n" \
  "v_mfma_f32_16x16x32_bf16 v[184:187], a[172:175], a[24:27], v[184:187]\n" \
  "v_mfma_f32_16x16x32_bf16 v[188:191], a[172:175], a[28:31], v[188:191]\n" \
  "s_waitcnt lgkmcnt(6)\n" \
  "v_mfma_f32_16x16x32_bf16 v[216:219], a[188:191], a[16:19], v[216:219]\n" \
  "ds_read_b64_tr_b16 a[176:177], v230 offset:32768\n" \
  "v_mfma_f32_16x16x32_bf16 v[220:223], a[188:191], a[20:23], v[220:223]\n" \
  "ds_read_b64_tr_b16 a[178:179], v230 offset:36864\n" \
  "s_waitcnt lgkmcnt(6)\n" \
  "v_mfma_f32_16x16x32_bf16 v[0:3], a[128:131], a[32:35], v[0:3]\n" \
  "s_add_u32 m0, s48, 65536\n" \
  "ds_read_b64_tr_b16 a[140:141], v225 offset:40960\n" \
  "v_mfma_f32_16x16x32_bf16 v[4:7], a[128:131], a[36:39], v[4:7]\n" \
  "ds_read_b64_tr_b16 a[142:143], v225 offset:45056\n" \
  "v_mfma_f32_16x16x32_bf16 v[8:11], a[128:131], a[40:43], v[8:11]\n" \
  "buffer_load_dwordx4 v240, s[36:39], s49 offen offset:0 lds\n" \
  "v_mfma_f32_16x16x32_bf16 v[12:15], a[128:131], a[44:47], v[12:15]\n" \
  "s_waitcnt lgkmcnt(6)\n" \
  "v_mfma_f32_16x16x32_bf16 v[16:19], a[132:135], a[32:35], v[16:19]\n" \
  "ds_read_b64_tr_b16 a[144:145], v226 offset:32768\n" \
  "v_mfma_f32_16x16x32_bf16 v[20:23], a[132:135], a[36:39], v[20:23]\n" \
  "ds_read_b64_tr_b16 a[146:147], v226 offset:36864\n" \
  "v_mfma_f32_16x16x32_bf16 v[24:27], a[132:135], a[40:43], v[24:27]\n" \
  "buffer_load_dwordx4 a[64:67], v244, s[40:43], s50 offen offset:0 sc1\n" \
  "v_mfma_f32_16x16x32_bf16 v[28:31], a[132:135], a[44:47], v[28:31]\n" \
  "s_waitcnt lgkmcnt(6)\n" \
  "v_mfma_f32_16x16x32_bf16 v[32:35], a[136:139], a[32:35], v[32:35]\n" \
  "ds_read_b64_tr_b16 a[148:149], v227 offset:32768\n" \
  "v_mfma_f32_16x16x32_bf16 v[36:39], a[136:139], a[36:39], v[36:39]\n" \
  "ds_read_b64_tr_b16 a[150:151], v227 offset:36864\n" \
  "v_mfma_f32_16x16x32_bf16 v[40:43], a[136:139], a[40:43], v[40:43]\n" \
  "buffer_load_dwordx4 v241, s[36:39], s49 offen offset:1024 lds\n" \
  "v_mfma_f32_16x16x32_bf16 v[44:47], a[136:139], a[44:47], v[44:47]\n" \
  "s_waitcnt lgkmcnt(6)\n" \
  "v_mfma_f32_16x16x32_bf16 v[192:195], a[176:179], a[32:35], v[192:195]\n" \
  "ds_read_b64_tr_b16 a[180:181], v231 offset:32768\n" \
  "v_mfma_f32_16x16x32_bf16 v[196:199], a[176:179], a[36:39], v[196:199]\n" \
  "ds_read_b64_tr_b16 a[182:183], v231 offset:36864\n" \
  "s_waitcnt lgkmcnt(6)\n" \
  "v_mfma_f32_16x16x32_bf16 v[48:51], a[140:143], a[32:35], v[48:51]\n" \
  "ds_read_b64_tr_b16 a[152:153], v226 offset:40960\n" \
  "buffer_load_dwordx4 a[68:71], v244, s[40:43], s50 offen offset:1024 sc1\n" \
  "v_mfma_f32_16x16x32_bf16 v[52:55], a[140:143], a[36:39], v[52:55]\n" \
  "ds_read_b64_tr_b16 a[154:155], v226 offset:45056\n" \
  "v_mfma_f32_16x16x32_bf16 v[56:59], a[140:143], a[40:43], v[56:59]\n" \
  "v_mfma_f32_16x16x32_bf16 v[60:63], a[140:143], a[44:47], v[60:63]\n" \
  "s_waitcnt lgkmcnt(6)\n" \
  "v_mfma_f32_16x16x32_bf16 v[64:67], a[144:147], a[32:35], v[64:67]\n" \
  "ds_read_b64_tr_b16 a[156:157], v227 offset:40960\n" \
  "buffer_load_dwordx4 v242, s[36:39], s49 offen offset:2048 lds\n" \
  "v_mfma_f32_16x16x32_bf16 v[68:71], a[144:147], a[36:39], v[68:71]\n" \
  "ds_read_b64_tr_b16 a[158:159], v227 offset:45056\n" \
  "v_mfma_f32_16x16x32_bf16 v[72:75], a[144:147], a[40:43], v[72:75]\n" \
  "v_mfma_f32_16x16x32_bf16 v[76:79], a[144:147], a[44:47], v[76:79]\n" \
  "s_waitcnt lgkmcnt(6)\n" \
  "v_mfma_f32_16x16x32_bf16 v[80:83], a[148:151], a[32:35], v[80:83]\n" \
  "ds_read_b64_tr_b16 a[160:161], v228 offset:32768\n" \
  "buffer_load_dwordx4 a[72:75], v245, s[40:43], s50 offen offset:0 sc1\n" \
  "v_mfma_f32_16x16x32_bf16 v[84:87], a[148:151], a[36:39], v[84:87]\n" \
  "ds_read_b64_tr_b16 a[162:163], v228 offset:36864\n" \
  "v_mfma_f32_16x16x32_bf16 v[88:91], a[148:151], a[40:43], v[88:91]\n" \
  "v_mfma_f32_16x16x32_bf16 v[92:95], a[148:151], a[44:47], v[92:95]\n" \
  "s_waitcnt lgkmcnt(6)\n" \
  "v_mfma_f32_16x16x32_bf16 v[200:203], a[180:183], a[32:35], v[200:203]\n" \
  "ds_read_b64_tr_b16 a[184:185], v230 offset:40960\n" \
  "buffer_load_dwordx4 v243, s[36:39], s49 offen offset:3072 lds\n" \
  "v_mfma_f32_16x16x32_bf16 v[204:207], a[180:183], a[36:39], v[204:207]\n" \
  "ds_read_b64_tr_b16 a[186:187], v230 offset:45056\n" \
  "s_waitcnt lgkmcnt(6)\n" \
  "v_mfma_f32_16x16x32_bf16 v[96:99], a[152:155], a[32:35], v[96:99]\n" \
  "ds_read_b64_tr_b16 a[164:165], v229 offset:32768\n" \
  "v_mfma_f32_16x16x32_bf16 v[100:103], a[152:155], a[36:39], v[100:103]\n" \
  "ds_read_b64_tr_b16 a[166:167], v229 offset:36864\n" \
  "v_mfma_f32_16x16x32_bf16 v[104:107], a[152:155], a[40:43], v[104:107]\n" \
  "buffer_load_dwordx4 a[76:79], v245, s[40:43], s50 offen offset:1024 sc1\n" \
  "v_mfma_f32_16x16x32_bf16 v[108:111], a[152:155], a[44:47], v[108:111]\n" \
  "s_add_u32 s49, s49, 229376\n" \
  "s_waitcnt lgkmcnt(6)\n" \
  "v_mfma_f32_16x16x32_bf16 v[112:115], a[156:159], a[32:35], v[112:115]\n" \
  "ds_read_b64_tr_b16 a[168:169], v228 offset:40960\n" \
  "s_add_u32 s50, s50, 4096\n" \
  "v_mfma_f32_16x16x32_bf16 v[116:119], a[156:159], a[36:39], v[116:119]\n" \
  "ds_read_b64_tr_b16 a[170:171], v228 offset:45056\n" \
  "v_mfma_f32_16x16x32_bf16 v[120:123], a[156:159], a[40:43], v[120:123]\n" \
  "v_mfma_f32_16x16x32_bf16 v[124:127], a[156:159], a[44:47], v[124:127]\n" \
  "s_waitcnt lgkmcnt(6)\n" \
  "v_mfma_f32_16x16x32_bf16 v[128:131], a[160:163], a[32:35], v[128:131]\n" \
  "ds_read_b64_tr_b16 a[172:173], v229 offset:40960\n" \
  "v_mfma_f32_16x16x32_bf16 v[132:135], a[160:163], a[36:39], v[132:135]\n" \
  "ds_read_b64_tr_b16 a[174:175], v229 offset:45056\n" \
  "v_mfma_f32_16x16x32_bf16 v[136:139], a[160:163], a[40:43], v[136:139]\n" \
  "v_mfma_f32_16x16x32_bf16 v[140:143], a[160:163], a[44:47], v[140:143]\n" \
  "s_waitcnt lgkmcnt(6)\n" \
  "v_mfma_f32_16x16x32_bf16 v[208:211], a[184:187], a[32:35], v[208:211]\n" \
  "ds_read_b64_tr_b16 a[188:189], v231 offset:40960\n" \
  "v_mfma_f32_16x16x32_bf16 v[212:215], a[184:187], a[36:39], v[212:215]\n" \
  "ds_read_b64_tr_b16 a[190:191], v231 offset:45056\n" \
  "s_waitcnt vmcnt(9)\n" \
  "s_barrier\n" \
  "s_waitcnt lgkmcnt(6)\n" \
  "v_mfma_f32_16x16x32_bf16 v[144:147], a[164:167], a[32:35], v[144:147]\n" \
  "ds_read_b64_tr_b16 a[128:129], v224 offset:49152\n" \
  "v_mfma_f32_16x16x32_bf16 v[148:151], a[164:167], a[36:39], v[148:151]\n" \
  "ds_read_b64_tr_b16 a[130:131], v224 offset:53248\n" \
  "v_mfma_f32_16x16x32_bf16 v[152:155], a[164:167], a[40:43], v[152:155]\n" \
  "v_mfma_f32_16x16x32_bf16 v[156:159], a[164:167], a[44:47], v[156:159]\n" \
  "s_waitcnt lgkmcnt(6)\n" \
  "v_mfma_f32_16x16x32_bf16 v[160:163], a[168:171], a[32:35], v[160:163]\n" \
  "ds_read_b64_tr_b16 a[132:133], v225 offset:49152\n" \
  "buffer_load_dword v247, v248, s[60:63], s79 offen\n" \
  "v_mfma_f32_16x16x32_bf16 v[164:167], a[168:171], a[36:39], v[164:167]\n" \
  "ds_read_b64_tr_b16 a[134:135], v225 offset:53248\n" \
  "v_mfma_f32_16x16x32_bf16 v[168:171], a[168:171], a[40:43], v[168:171]\n" \
  "s_add_u32 s79, s79, 458752\n" \
  "v_mfma_f32_16x16x32_bf16 v[172:175], a[168:171], a[44:47], v[172:175]\n" \
  "s_waitcnt lgkmcnt(6)\n" \
  "v_mfma_f32_16x16x32_bf16 v[176:179], a[172:175], a[32:35], v[176:179]\n" \
  "ds_read_b64_tr_b16 a[136:137], v224 offset:57344\n" \
  "v_mfma_f32_16x16x32_bf16 v[180:183], a[172:175], a[36:39], v[180:183]\n" \
  "ds_read_b64_tr_b16 a[138:139], v224 offset:61440\n" \
  "v_mfma_f32_16x16x32_bf16 v[184:187], a[172:175], a[40:43], v[184:187]\n" \
  "v_mfma_f32_16x16x32_bf16 v[188:191], a[172:175], a[44:47], v[188:191]\n" \
  "s_waitcnt lgkmcnt(6)\n" \
  "v_mfma_f32_16x16x32_bf16 v[216:219], a[188:191], a[32:35], v[216:219]\n" \
  "ds_read_b64_tr_b16 a[176:177], v230 offset:49152\n" \
  "v_mfma_f32_16x16x32_bf16 v[220:223], a[188:191], a[36:39], v[220:223]\n" \
  "ds_read_b64_tr_b16 a[178:179], v230 offset:53248\n" \
  "s_waitcnt lgkmcnt(6)\n" \
  "v_mfma_f32_16x16x32_bf16 v[0:3], a[128:131], a[48:51], v[0:3]\n" \
  "s_add_u32 m0, s48, 81920\n" \
  "ds_read_b64_tr_b16 a[140:141], v225 offset:57344\n" \
  "v_mfma_f32_16x16x32_bf16 v[4:7], a[128:131], a[52:55], v[4:7]\n" \
  "ds_read_b64_tr_b16 a[142:143], v225 offset:61440\n" \
  "v_mfma_f32_16x16x32_bf16 v[8:11], a[128:131], a[56:59], v[8:11]\n" \
  "buffer_load_dwordx4 v240, s[36:39], s49 offen offset:0 lds\n" \
  "v_mfma_f32_16x16x32_bf16 v[12:15], a[128:131], a[60:63], v[12:15]\n" \
  "s_waitcnt lgkmcnt(6)\n" \
  "v_mfma_f32_16x16x32_bf16 v[16:19], a[132:135], a[48:51], v[16:19]\n" \
  "ds_read_b64_tr_b16 a[144:145], v226 offset:49152\n" \
  "v_mfma_f32_16x16x32_bf16 v[20:23], a[132:135], a[52:55], v[20:23]\n" \
  "ds_read_b64_tr_b16 a[146:147], v226 offset:53248\n" \
  "v_mfma_f32_16x16x32_bf16 v[24:27], a[132:135], a[56:59], v[24:27]\n" \
  "buffer_load_dwordx4 a[80:83], v244, s[40:43], s50 offen offset:0 sc1\n" \
  "v_mfma_f32_16x16x32_bf16 v[28:31], a[132:135], a[60:63], v[28:31]\n" \
  "s_waitcnt lgkmcnt(6)\n" \
  "v_mfma_f32_16x16x32_bf16 v[32:35], a[136:139], a[48:51], v[32:35]\n" \
  "ds_read_b64_tr_b16 a[148:149], v227 offset:49152\n" \
  "v_mfma_f32_16x16x32_bf16 v[36:39], a[136:139], a[52:55], v[36:39]\n" \
  "ds_read_b64_tr_b16 a[150:151], v227 offset:53248\n" \
  "v_mfma_f32_16x16x32_bf16 v[40:43], a[136:139], a[56:59], v[40:43]\n" \
  "buffer_load_dwordx4 v241, s[36:39], s49 offen offset:1024 lds\n" \
  "v_mfma_f32_16x16x32_bf16 v[44:47], a[136:139], a[60:63], v[44:47]\n" \
  "s_waitcnt lgkmcnt(6)\n" \
  "v_mfma_f32_16x16x32_bf16 v[192:195], a[176:179], a[48:51], v[192:195]\n" \
  "ds_read_b64_tr_b16 a[180:181], v231 offset:49152\n" \
  "v_mfma_f32_16x16x32_bf16 v[196:199], a[176:179], a[52:55], v[196:199]\n" \
  "ds_read_b64_tr_b16 a[182:183], v231 offset:53248\n" \
  "s_waitcnt lgkmcnt(6)\n" \
  "v_mfma_f32_16x16x32_bf16 v[48:51], a[140:143], a[48:51], v[48:51]\n" \
  "ds_read_b64_tr_b16 a[152:153], v226 offset:57344\n" \
  "buffer_load_dwordx4 a[84:87], v244, s[40:43], s50 offen offset:1024 sc1\n" \
  "v_mfma_f32_16x16x32_bf16 v[52:55], a[140:143], a[52:55], v[52:55]\n" \
  "ds_read_b64_tr_b16 a[154:155], v226 offset:61440\n" \
  "v_mfma_f32_16x16x32_bf16 v[56:59], a[140:143], a[56:59], v[56:59]\n" \
  "v_mfma_f32_16x16x32_bf16 v[60:63], a[140:143], a[60:63], v[60:63]\n" \
  "s_waitcnt lgkmcnt(6)\n" \
  "v_mfma_f32_16x16x32_bf16 v[64:67], a[144:147], a[48:51], v[64:67]\n" \
  "ds_read_b64_tr_b16 a[156:157], v227 offset:57344\n" \
  "buffer_load_dwordx4 v242, s[36:39], s49 offen offset:2048 lds\n" \
  "v_mfma_f32_16x16x32_bf16 v[68:71], a[144:147], a[52:55], v[68:71]\n" \
  "ds_read_b64_tr_b16 a[158:159], v227 offset:61440\n" \
  "v_mfma_f32_16x16x32_bf16 v[72:75], a[144:147], a[56:59], v[72:75]\n" \
  "v_mfma_f32_16x16x32_bf16 v[76:79], a[144:147], a[60:63], v[76:79]\n" \
  "s_waitcnt lgkmcnt(6)\n" \
  "v_mfma_f32_16x16x32_bf16 v[80:83], a[148:151], a[48:51], v[80:83]\n" \
  "ds_read_b64_tr_b16 a[160:161], v228 offset:49152\n" \
  "buffer_load_dwordx4 a[88:91], v245, s[40:43], s50 offen offset:0 sc1\n" \
  "v_mfma_f32_16x16x32_bf16 v[84:87], a[148:151], a[52:55], v[84:87]\n" \
  "ds_read_b64_tr_b16 a[162:163], v228 offset:53248\n" \
  "v_mfma_f32_16x16x32_bf16 v[88:91], a[148:151], a[56:59], v[88:91]\n" \
  "v_mfma_f32_16x16x32_bf16 v[92:95], a[148:151], a[60:63], v[92:95]\n" \
  "s_waitcnt lgkmcnt(6)\n" \
  "v_mfma_f32_16x16x32_bf16 v[200:203], a[180:183], a[48:51], v[200:203]\n" \
  "ds_read_b64_tr_b16 a[184:185], v230 offset:57344\n" \
  "buffer_load_dwordx4 v243, s[36:39], s49 offen offset:3072 lds\n" \
  "v_mfma_f32_16x16x32_bf16 v[204:207], a[180:183], a[52:55], v[204:207]\n" \
  "ds_read_b64_tr_b16 a[186:187], v230 offset:61440\n" \
  "s_waitcnt lgkmcnt(6)\n" \
  "v_mfma_f32_16x16x32_bf16 v[96:99], a[152:155], a[48:51], v[96:99]\n" \
  "ds_read_b64_tr_b16 a[164:165], v229 offset:49152\n" \
  "v_mfma_f32_16x16x32_bf16 v[100:103], a[152:155], a[52:55], v[100:103]\n" \
  "ds_read_b64_tr_b16 a[166:167], v229 offset:53248\n" \
  "v_mfma_f32_16x16x32_bf16 v[104:107], a[152:155], a[56:59], v[104:107]\n" \
  "buffer_load_dwordx4 a[92:95], v245, s[40:43], s50 offen offset:1024 sc1\n" \
  "v_mfma_f32_16x16x32_bf16 v[108:111], a[152:155], a[60:63], v[108:111]\n" \
  "s_add_u32 s49, s49, 229376\n" \
  "s_waitcnt lgkmcnt(6)\n" \
  "v_mfma_f32_16x16x32_bf16 v[112:115], a[156:159], a[48:51], v[112:115]\n" \
  "ds_read_b64_tr_b16 a[168:169], v228 offset:57344\n" \
  "s_add_u32 s50, s50, 4096\n" \
  "v_mfma_f32_16x16x32_bf16 v[116:119], a[156:159], a[52:55], v[116:119]\n" \
  "ds_read_b64_tr_b16 a[170:171], v228 offset:61440\n" \
  "v_mfma_f32_16x16x32_bf16 v[120:123], a[156:159], a[56:59], v[120:123]\n" \
  "v_mfma_f32_16x16x32_bf16 v[124:127], a[156:159], a[60:63], v[124:127]\n" \
  "s_waitcnt lgkmcnt(6)\n" \
  "v_mfma_f32_16x16x32_bf16 v[128:131], a[160:163], a[48:51], v[128:131]\n" \
  "ds_read_b64_tr_b16 a[172:173], v229 offset:57344\n" \
  "v_mfma_f32_16x16x32_bf16 v[132:135], a[160:163], a[52:55], v[132:135]\n" \
  "ds_read_b64_tr_b16 a[174:175], v229 offset:61440\n" \
  "v_mfma_f32_16x16x32_bf16 v[136:139], a[160:163], a[56:59], v[136:139]\n" \
  "v_mfma_f32_16x16x32_bf16 v[140:143], a[160:163], a[60:63], v[140:143]\n" \
  "s_waitcnt lgkmcnt(6)\n" \
  "v_mfma_f32_16x16x32_bf16 v[208:211], a[184:187], a[48:51], v[208:211]\n" \
  "ds_read_b64_tr_b16 a[188:189], v231 offset:57344\n" \
  "v_mfma_f32_16x16x32_bf16 v[212:215], a[184:187], a[52:55], v[212:215]\n" \
  "ds_read_b64_tr_b16 a[190:191], v231 offset:61440\n" \
  "s_waitcnt vmcnt(9)\n" \
  "s_barrier\n" \
  "s_waitcnt lgkmcnt(6)\n" \
  "v_mfma_f32_16x16x32_bf16 v[144:147], a[164:167], a[48:51], v[144:147]\n" \
  "ds_read_b64_tr_b16 a[128:129], v232 offset:0\n" \
  "v_mfma_f32_16x16x32_bf16 v[148:151], a[164:167], a[52:55], v[148:151]\n" \
  "ds_read_b64_tr_b16 a[130:131], v232 offset:4096\n" \
  "v_mfma_f32_16x16x32_bf16 v[152:155], a[164:167], a[56:59], v[152:155]\n" \
  "buffer_load_dword v250, v249, s[84:87], s88 offen\n" \
  "v_mfma_f32_16x16x32_bf16 v[156:159], a[164:167], a[60:63], v[156:159]\n" \
  "s_waitcnt lgkmcnt(6)\n" \
  "v_mfma_f32_16x16x32_bf16 v[160:163], a[168:171], a[48:51], v[160:163]\n" \
  "ds_read_b64_tr_b16 a[132:133], v233 offset:0\n" \
  "v_mfma_f32_16x16x32_bf16 v[164:167], a[168:171], a[52:55], v[164:167]\n" \
  "ds_read_b64_tr_b16 a[134:135], v233 offset:4096\n" \
  "v_mfma_f32_16x16x32_bf16 v[168:171], a[168:171], a[56:59], v[168:171]\n" \
  "v_mfma_f32_16x16x32_bf16 v[172:175], a[168:171], a[60:63], v[172:175]\n" \
  "s_waitcnt lgkmcnt(6)\n" \
  "v_mfma_f32_16x16x32_bf16 v[176:179], a[172:175], a[48:51], v[176:179]\n" \
  "ds_read_b64_tr_b16 a[136:137], v232 offset:8192\n" \
  "v_mfma_f32_16x16x32_bf16 v[180:183], a[172:175], a[52:55], v[180:183]\n" \
  "ds_read_b64_tr_b16 a[138:139], v232 offset:12288\n" \
  "v_mfma_f32_16x16x32_bf16 v[184:187], a[172:175], a[56:59], v[184:187]\n" \
  "v_mfma_f32_16x16x32_bf16 v[188:191], a[172:175], a[60:63], v[188:191]\n" \
  "s_waitcnt lgkmcnt(6)\n" \
  "v_mfma_f32_16x16x32_bf16 v[216:219], a[188:191], a[48:51], v[216:219]\n" \
  "ds_read_b64_tr_b16 a[176:177], v238 offset:0\n" \
  "v_mfma_f32_16x16x32_bf16 v[220:223], a[188:191], a[52:55], v[220:223]\n" \
  "ds_read_b64_tr_b16 a[178:179], v238 offset:4096\n" \
  "s_waitcnt lgkmcnt(6)\n" \
  "v_mfma_f32_16x16x32_bf16 v[0:3], a[128:131], a[64:67], v[0:3]\n" \
  "s_add_u32 m0, s48, 98304\n" \
  "ds_read_b64_tr_b16 a[140:141], v233 offset:8192\n" \
  "v_mfma_f32_16x16x32_bf16 v[4:7], a[128:131], a[68:71], v[4:7]\n" \
  "ds_read_b64_tr_b16 a[142:143], v233 offset:12288\n" \
  "v_mfma_f32_16x16x32_bf16 v[8:11], a[128:131], a[72:75], v[8:11]\n" \
  "buffer_load_dwordx4 v240, s[36:39], s49 offen offset:0 lds\n" \
  "v_mfma_f32_16x16x32_bf16 v[12:15], a[128:131], a[76:79], v[12:15]\n" \
  "s_waitcnt lgkmcnt(6)\n" \
  "v_mfma_f32_16x16x32_bf16 v[16:19], a[132:135], a[64:67], v[16:19]\n" \
  "ds_read_b64_tr_b16 a[144:145], v234 offset:0\n" \
  "v_mfma_f32_16x16x32_bf16 v[20:23], a[132:135], a[68:71], v[20:23]\n" \
  "ds_read_b64_tr_b16 a[146:147], v234 offset:4096\n" \
  "v_mfma_f32_16x16x32_bf16 v[24:27], a[132:135], a[72:75], v[24:27]\n" \
  "buffer_load_dwordx4 a[96:99], v244, s[40:43], s50 offen offset:0 sc1\n" \
  "v_mfma_f32_16x16x32_bf16 v[28:31], a[132:135], a[76:79], v[28:31]\n" \
  "s_waitcnt lgkmcnt(6)\n" \
  "v_mfma_f32_16x16x32_bf16 v[32:35], a[136:139], a[64:67], v[32:35]\n" \
  "ds_read_b64_tr_b16 a[148:149], v235 offset:0\n" \
  "v_mfma_f32_16x16x32_bf16 v[36:39], a[136:139], a[68:71], v[36:39]\n" \
  "ds_read_b64_tr_b16 a[150:151], v235 offset:4096\n" \
  "v_mfma_f32_16x16x32_bf16 v[40:43], a[136:139], a[72:75], v[40:43]\n" \
  "buffer_load_dwordx4 v241, s[36:39], s49 offen offset:1024 lds\n" \
  "v_mfma_f32_16x16x32_bf16 v[44:47], a[136:139], a[76:79], v[44:47]\n" \
  "s_waitcnt lgkmcnt(6)\n" \
  "v_mfma_f32_16x16x32_bf16 v[192:195], a[176:179], a[64:67], v[192:195]\n" \
  "ds_read_b64_tr_b16 a[180:181], v239 offset:0\n" \
  "v_mfma_f32_16x16x32_bf16 v[196:199], a[176:179], a[68:71], v[196:199]\n" \
  "ds_read_b64_tr_b16 a[182:183], v239 offset:4096\n" \
  "s_waitcnt lgkmcnt(6)\n" \
  "v_mfma_f32_16x16x32_bf16 v[48:51], a[140:143], a[64:67], v[48:51]\n" \
  "ds_read_b64_tr_b16 a[152:153], v234 offset:8192\n" \
  "buffer_load_dwordx4 a[100:103], v244, s[40:43], s50 offen offset:1024 sc1\n" \
  "v_mfma_f32_16x16x32_bf16 v[52:55], a[140:143], a[68:71], v[52:55]\n" \
  "ds_read_b64_tr_b16 a[154:155], v234 offset:12288\n" \
  "v_mfma_f32_16x16x32_bf16 v[56:59], a[140:143], a[72:75], v[56:59]\n" \
  "v_mfma_f32_16x16x32_bf16 v[60:63], a[140:143], a[76:79], v[60:63]\n" \
  "s_waitcnt lgkmcnt(6)\n" \
  "v_mfma_f32_16x16x32_bf16 v[64:67], a[144:147], a[64:67], v[64:67]\n" \
  "ds_read_b64_tr_b16 a[156:157], v235 offset:8192\n" \
  "buffer_load_dwordx4 v242, s[36:39], s49 offen offset:2048 lds\n" \
  "v_mfma_f32_16x16x32_bf16 v[68:71], a[144:147], a[68:71], v[68:71]\n" \
  "ds_read_b64_tr_b16 a[158:159], v235 offset:12288\n" \
  "v_mfma_f32_16x16x32_bf16 v[72:75], a[144:147], a[72:75], v[72:75]\n" \
  "v_mfma_f32_16x16x32_bf16 v[76:79], a[144:147], a[76:79], v[76:79]\n" \
  "s_waitcnt lgkmcnt(6)\n" \
  "v_mfma_f32_16x16x32_bf16 v[80:83], a[148:151], a[64:67], v[80:83]\n" \
  "ds_read_b64_tr_b16 a[160:161], v236 offset:0\n" \
  "buffer_load_dwordx4 a[104:107], v245, s[40:43], s50 offen offset:0 sc1\n" \
  "v_mfma_f32_16x16x32_bf16 v[84:87], a[148:151], a[68:71], v[84:87]\n" \
  "ds_read_b64_tr_b16 a[162:163], v236 offset:4096\n" \
  "v_mfma_f32_16x16x32_bf16 v[88:91], a[148:151], a[72:75], v[88:91]\n" \
  "v_mfma_f32_16x16x32_bf16 v[92:95], a[148:151], a[76:79], v[92:95]\n" \
  "s_waitcnt lgkmcnt(6)\n" \
  "v_mfma_f32_16x16x32_bf16 v[200:203], a[180:183], a[64:67], v[200:203]\n" \
  "ds_read_b64_tr_b16 a[184:185], v238 offset:8192\n" \
  "buffer_load_dwordx4 v243, s[36:39], s49 offen offset:3072 lds\n" \
  "v_mfma_f32_16x16x32_bf16 v[204:207], a[180:183], a[68:71], v[204:207]\n" \
  "ds_read_b64_tr_b16 a[186:187], v238 offset:12288\n" \
  "s_waitcnt lgkmcnt(6)\n" \
  "v_mfma_f32_16x16x32_bf16 v[96:99], a[152:155], a[64:67], v[96:99]\n" \
  "ds_read_b64_tr_b16 a[164:165], v237 offset:0\n" \
  "v_mfma_f32_16x16x32_bf16 v[100:103], a[152:155], a[68:71], v[100:103]\n" \
  "ds_read_b64_tr_b16 a[166:167], v237 offset:4096\n" \
  "v_mfma_f32_16x16x32_bf16 v[104:107], a[152:155], a[72:75], v[104:107]\n" \
  "buffer_load_dwordx4 a[108:111], v245, s[40:43], s50 offen offset:1024 sc1\n" \
  "v_mfma_f32_16x16x32_bf16 v[108:111], a[152:155], a[76:79], v[108:111]\n" \
  "s_add_u32 s49, s49, 229376\n" \
  "s_waitcnt lgkmcnt(6)\n" \
  "v_mfma_f32_16x16x32_bf16 v[112:115], a[156:159], a[64:67], v[112:115]\n" \
  "ds_read_b64_tr_b16 a[168:169], v236 offset:8192\n" \
  "s_add_u32 s50, s50, 4096\n" \
  "v_mfma_f32_16x16x32_bf16 v[116:119], a[156:159], a[68:71], v[116:119]\n" \
  "ds_read_b64_tr_b16 a[170:171], v236 offset:12288\n" \
  "v_mfma_f32_16x16x32_bf16 v[120:123], a[156:159], a[72:75], v[120:123]\n" \
  "v_mfma_f32_16x16x32_bf16 v[124:127], a[156:159], a[76:79], v[124:127]\n" \
  "s_waitcnt lgkmcnt(6)\n" \
  "v_mfma_f32_16x16x32_bf16 v[128:131], a[160:163], a[64:67], v[128:131]\n" \
  "ds_read_b64_tr_b16 a[172:173], v237 offset:8192\n" \
  "v_mfma_f32_16x16x32_bf16 v[132:135], a[160:163], a[68:71], v[132:135]\n" \
  "ds_read_b64_tr_b16 a[174:175], v237 offset:12288\n" \
  "v_mfma_f32_16x16x32_bf16 v[136:139], a[160:163], a[72:75], v[136:139]\n" \
  "v_mfma_f32_16x16x32_bf16 v[140:143], a[160:163], a[76:79], v[140:143]\n" \
  "s_waitcnt lgkmcnt(6)\n" \
  "v_mfma_f32_16x16x32_bf16 v[208:211], a[184:187], a[64:67], v[208:211]\n" \
  "ds_read_b64_tr_b16 a[188:189], v239 offset:8192\n" \
  "v_mfma_f32_16x16x32_bf16 v[212:215], a[184:187], a[68:71], v[212:215]\n" \
  "ds_read_b64_tr_b16 a[190:191], v239 offset:12288\n" \
  "s_waitcnt vmcnt(9)\n" \
  "s_barrier\n" \
  "s_waitcnt lgkmcnt(6)\n" \
  "v_mfma_f32_16x16x32_bf16 v[144:147], a[164:167], a[64:67], v[144:147]\n" \
  "ds_read_b64_tr_b16 a[128:129], v232 offset:16384\n" \
  "v_mfma_f32_16x16x32_bf16 v[148:151], a[164:167], a[68:71], v[148:151]\n" \
  "ds_read_b64_tr_b16 a[130:131], v232 offset:20480\n" \
  "v_mfma_f32_16x16x32_bf16 v[152:155], a[164:167], a[72:75], v[152:155]\n" \
  "v_mfma_f32_16x16x32_bf16 v[156:159], a[164:167], a[76:79], v[156:159]\n" \
  "s_waitcnt lgkmcnt(6)\n" \
  "v_mfma_f32_16x16x32_bf16 v[160:163], a[168:171], a[64:67], v[160:163]\n" \
  "ds_read_b64_tr_b16 a[132:133], v233 offset:16384\n" \
  "buffer_load_dword v247, v248, s[60:63], s79 offen\n" \
  "v_mfma_f32_16x16x32_bf16 v[164:167], a[168:171], a[68:71], v[164:167]\n" \
  "ds_read_b64_tr_b16 a[134:135], v233 offset:20480\n" \
  "v_mfma_f32_16x16x32_bf16 v[168:171], a[168:171], a[72:75], v[168:171]\n" \
  "s_add_u32 s79, s79, 458752\n" \
  "v_mfma_f32_16x16x32_bf16 v[172:175], a[168:171], a[76:79], v[172:175]\n" \
  "s_waitcnt lgkmcnt(6)\n" \
  "v_mfma_f32_16x16x32_bf16 v[176:179], a[172:175], a[64:67], v[176:179]\n" \
  "ds_read_b64_tr_b16 a[136:137], v232 offset:24576\n" \
  "v_mfma_f32_16x16x32_bf16 v[180:183], a[172:175], a[68:71], v[180:183]\n" \
  "ds_read_b64_tr_b16 a[138:139], v232 offset:28672\n" \
  "v_mfma_f32_16x16x32_bf16 v[184:187], a[172:175], a[72:75], v[184:187]\n" \
  "v_mfma_f32_16x16x32_bf16 v[188:191], a[172:175], a[76:79], v[188:191]\n" \
  "s_waitcnt lgkmcnt(6)\n" \
  "v_mfma_f32_16x16x32_bf16 v[216:219], a[188:191], a[64:67], v[216:219]\n" \
  "ds_read_b64_tr_b16 a[176:177], v238 offset:16384\n" \
  "v_mfma_f32_16x16x32_bf16 v[220:223], a[188:191], a[68:71], v[220:223]\n" \
  "ds_read_b64_tr_b16 a[178:179], v238 offset:20480\n" \
  "s_waitcnt lgkmcnt(6)\n" \
  "v_mfma_f32_16x16x32_bf16 v[0:3], a[128:131], a[80:83], v[0:3]\n" \
  "s_add_u32 m0, s48, 114688\n" \
  "ds_read_b64_tr_b16 a[140:141], v233 offset:24576\n" \
  "v_mfma_f32_16x16x32_bf16 v[4:7], a[128:131], a[84:87], v[4:7]\n" \
  "ds_read_b64_tr_b16 a[142:143], v233 offset:28672\n" \
  "v_mfma_f32_16x16x32_bf16 v[8:11], a[128:131], a[88:91], v[8:11]\n" \
  "buffer_load_dwordx4 v240, s[36:39], s49 offen offset:0 lds\n" \
  "v_mfma_f32_16x16x32_bf16 v[12:15], a[128:131], a[92:95], v[12:15]\n" \
  "s_waitcnt lgkmcnt(6)\n" \
  "v_mfma_f32_16x16x32_bf16 v[16:19], a[132:135], a[80:83], v[16:19]\n" \
  "ds_read_b64_tr_b16 a[144:145], v234 offset:16384\n" \
  "v_mfma_f32_16x16x32_bf16 v[20:23], a[132:135], a[84:87], v[20:23]\n" \
  "ds_read_b64_tr_b16 a[146:147], v234 offset:20480\n" \
  "v_mfma_f32_16x16x32_bf16 v[24:27], a[132:135], a[88:91], v[24:27]\n" \
  "buffer_load_dwordx4 a[112:115], v244, s[40:43], s50 offen offset:0 sc1\n" \
  "v_mfma_f32_16x16x32_bf16 v[28:31], a[132:135], a[92:95], v[28:31]\n" \
  "s_waitcnt lgkmcnt(6)\n" \
  "v_mfma_f32_16x16x32_bf16 v[32:35], a[136:139], a[80:83], v[32:35]\n" \
  "ds_read_b64_tr_b16 a[148:149], v235 offset:16384\n" \
  "v_mfma_f32_16x16x32_bf16 v[36:39], a[136:139], a[84:87], v[36:39]\n" \
  "ds_read_b64_tr_b16 a[150:151], v235 offset:20480\n" \
  "v_mfma_f32_16x16x32_bf16 v[40:43], a[136:139], a[88:91], v[40:43]\n" \
  "buffer_load_dwordx4 v241, s[36:39], s49 offen offset:1024 lds\n" \
  "v_mfma_f32_16x16x32_bf16 v[44:47], a[136:139], a[92:95], v[44:47]\n" \
  "s_waitcnt lgkmcnt(6)\n" \
  "v_mfma_f32_16x16x32_bf16 v[192:195], a[176:179], a[80:83], v[192:195]\n" \
  "ds_read_b64_tr_b16 a[180:181], v239 offset:16384\n" \
  "v_mfma_f32_16x16x32_bf16 v[196:199], a[176:179], a[84:87], v[196:199]\n" \
  "ds_read_b64_tr_b16 a[182:183], v239 offset:20480\n" \
  "s_waitcnt lgkmcnt(6)\n" \
  "v_mfma_f32_16x16x32_bf16 v[48:51], a[140:143], a[80:83], v[48:51]\n" \
  "ds_read_b64_tr_b16 a[152:153], v234 offset:24576\n" \
  "buffer_load_dwordx4 a[116:119], v244, s[40:43], s50 offen offset:1024 sc1\n" \
  "v_mfma_f32_16x16x32_bf16 v[52:55], a[140:143], a[84:87], v[52:55]\n" \
  "ds_read_b64_tr_b16 a[154:155], v234 offset:28672\n" \
  "v_mfma_f32_16x16x32_bf16 v[56:59], a[140:143], a[88:91], v[56:59]\n" \
  "v_mfma_f32_16x16x32_bf16 v[60:63], a[140:143], a[92:95], v[60:63]\n" \
  "s_waitcnt lgkmcnt(6)\n" \
  "v_mfma_f32_16x16x32_bf16 v[64:67], a[144:147], a[80:83], v[64:67]\n" \
  "ds_read_b64_tr_b16 a[156:157], v235 offset:24576\n" \
  "buffer_load_dwordx4 v242, s[36:39], s49 offen offset:2048 lds\n" \
  "v_mfma_f32_16x16x32_bf16 v[68:71], a[144:147], a[84:87], v[68:71]\n" \
  "ds_read_b64_tr_b16 a[158:159], v235 offset:28672\n" \
  "v_mfma_f32_16x16x32_bf16 v[72:75], a[144:147], a[88:91], v[72:75]\n" \
  "v_mfma_f32_16x16x32_bf16 v[76:79], a[144:147], a[92:95], v[76:79]\n" \
  "s_waitcnt lgkmcnt(6)\n" \
  "v_mfma_f32_16x16x32_bf16 v[80:83], a[148:151], a[80:83], v[80:83]\n" \
  "ds_read_b64_tr_b16 a[160:161], v236 offset:16384\n" \
  "buffer_load_dwordx4 a[120:123], v245, s[40:43], s50 offen offset:0 sc1\n" \
  "v_mfma_f32_16x16x32_bf16 v[84:87], a[148:151], a[84:87], v[84:87]\n" \
  "ds_read_b64_tr_b16 a[162:163], v236 offset:20480\n" \
  "v_mfma_f32_16x16x32_bf16 v[88:91], a[148:151], a[88:91], v[88:91]\n" \
  "v_mfma_f32_16x16x32_bf16 v[92:95], a[148:151], a[92:95], v[92:95]\n" \
  "s_waitcnt lgkmcnt(6)\n" \
  "v_mfma_f32_16x16x32_bf16 v[200:203], a[180:183], a[80:83], v[200:203]\n" \
  "ds_read_b64_tr_b16 a[184:185], v238 offset:24576\n" \
  "buffer_load_dwordx4 v243, s[36:39], s49 offen offset:3072 lds\n" \
  "v_mfma_f32_16x16x32_bf16 v[204:207], a[180:183], a[84:87], v[204:207]\n" \
  "ds_read_b64_tr_b16 a[186:187], v238 offset:28672\n" \
  "s_waitcnt lgkmcnt(6)\n" \
  "v_mfma_f32_16x16x32_bf16 v[96:99], a[152:155], a[80:83], v[96:99]\n" \
  "ds_read_b64_tr_b16 a[164:165], v237 offset:16384\n" \
  "v_mfma_f32_16x16x32_bf16 v[100:103], a[152:155], a[84:87], v[100:103]\n" \
  "ds_read_b64_tr_b16 a[166:167], v237 offset:20480\n" \
  "v_mfma_f32_16x16x32_bf16 v[104:107], a[152:155], a[88:91], v[104:107]\n" \
  "buffer_load_dwordx4 a[124:127], v245, s[40:43], s50 offen offset:1024 sc1\n" \
  "v_mfma_f32_16x16x32_bf16 v[108:111], a[152:155], a[92:95], v[108:111]\n" \
  "s_add_u32 s49, s49, 229376\n" \
  "s_waitcnt lgkmcnt(6)\n" \
  "v_mfma_f32_16x16x32_bf16 v[112:115], a[156:159], a[80:83], v[112:115]\n" \
  "ds_read_b64_tr_b16 a[168:169], v236 offset:24576\n" \
  "s_add_u32 s50, s50, 4096\n" \
  "v_mfma_f32_16x16x32_bf16 v[116:119], a[156:159], a[84:87], v[116:119]\n" \
  "ds_read_b64_tr_b16 a[170:171], v236 offset:28672\n" \
  "v_mfma_f32_16x16x32_bf16 v[120:123], a[156:159], a[88:91], v[120:123]\n" \
  "v_mfma_f32_16x16x32_bf16 v[124:127], a[156:159], a[92:95], v[124:127]\n" \
  "s_waitcnt lgkmcnt(6)\n" \
  "v_mfma_f32_16x16x32_bf16 v[128:131], a[160:163], a[80:83], v[128:131]\n" \
  "ds_read_b64_tr_b16 a[172:173], v237 offset:24576\n" \
  "v_mfma_f32_16x16x32_bf16 v[132:135], a[160:163], a[84:87], v[132:135]\n" \
  "ds_read_b64_tr_b16 a[174:175], v237 offset:28672\n" \
  "v_mfma_f32_16x16x32_bf16 v[136:139], a[160:163], a[88:91], v[136:139]\n" \
  "v_mfma_f32_16x16x32_bf16 v[140:143], a[160:163], a[92:95], v[140:143]\n" \
  "s_waitcnt lgkmcnt(6)\n" \
  "v_mfma_f32_16x16x32_bf16 v[208:211], a[184:187], a[80:83], v[208:211]\n" \
  "ds_read_b64_tr_b16 a[188:189], v239 offset:24576\n" \
  "v_mfma_f32_16x16x32_bf16 v[212:215], a[184:187], a[84:87], v[212:215]\n" \
  "ds_read_b64_tr_b16 a[190:191], v239 offset:28672\n" \
  "s_waitcnt vmcnt(9)\n" \
  "s_barrier\n" \
  "s_waitcnt lgkmcnt(6)\n" \
  "v_mfma_f32_16x16x32_bf16 v[144:147], a[164:167], a[80:83], v[144:147]\n" \
  "ds_read_b64_tr_b16 a[128:129], v232 offset:32768\n" \
  "v_mfma_f32_16x16x32_bf16 v[148:151], a[164:167], a[84:87], v[148:151]\n" \
  "ds_read_b64_tr_b16 a[130:131], v232 offset:36864\n" \
  "buffer_load_dword v247, v248, s[80:83], s78 offen\n" \
  "v_mfma_f32_16x16x32_bf16 v[152:155], a[164:167], a[88:91], v[152:155]\n" \
  "v_mfma_f32_16x16x32_bf16 v[156:159], a[164:167], a[92:95], v[156:159]\n" \
  "s_add_u32 s78, s78, 1024\n" \
  "s_waitcnt lgkmcnt(6)\n" \
  "v_mfma_f32_16x16x32_bf16 v[160:163], a[168:171], a[80:83], v[160:163]\n" \
  "ds_read_b64_tr_b16 a[132:133], v233 offset:32768\n" \
  "v_mfma_f32_16x16x32_bf16 v[164:167], a[168:171], a[84:87], v[164:167]\n" \
  "ds_read_b64_tr_b16 a[134:135], v233 offset:36864\n" \
  "v_mfma_f32_16x16x32_bf16 v[168:171], a[168:171], a[88:91], v[168:171]\n" \
  "v_mfma_f32_16x16x32_bf16 v[172:175], a[168:171], a[92:95], v[172:175]\n" \
  "s_waitcnt lgkmcnt(6)\n" \
  "v_mfma_f32_16x16x32_bf16 v[176:179], a[172:175], a[80:83], v[176:179]\n" \
  "ds_read_b64_tr_b16 a[136:137], v232 offset:40960\n" \
  "v_mfma_f32_16x16x32_bf16 v[180:183], a[172:175], a[84:87], v[180:183]\n" \
  "ds_read_b64_tr_b16 a[138:139], v232 offset:45056\n" \
  "v_mfma_f32_16x16x32_bf16 v[184:187], a[172:175], a[88:91], v[184:187]\n" \
  "v_mfma_f32_16x16x32_bf16 v[188:191], a[172:175], a[92:95], v[188:191]\n" \
  "s_waitcnt lgkmcnt(6)\n" \
  "v_mfma_f32_16x16x32_bf16 v[216:219], a[188:191], a[80:83], v[216:219]\n" \
  "ds_read_b64_tr_b16 a[176:177], v238 offset:32768\n" \
  "v_mfma_f32_16x16x32_bf16 v[220:223], a[188:191], a[84:87], v[220:223]\n" \
  "ds_read_b64_tr_b16 a[178:179], v238 offset:36864\n" \
  "s_waitcnt lgkmcnt(6)\n" \
  "v_mfma_f32_16x16x32_bf16 v[0:3], a[128:131], a[96:99], v[0:3]\n" \
  "s_add_u32 m0, s48, 0\n" \
  "s_cmp_eq_u32 s51, 3\n" \
  "ds_read_b64_tr_b16 a[140:141], v233 offset:40960\n" \
  "v_mfma_f32_16x16x32_bf16 v[4:7], a[128:131], a[100:103], v[4:7]\n" \
  "s_cselect_b32 s38, 0, s38\n" \
  "s_cselect_b32 s42, 0, s42\n" \
  "ds_read_b64_tr_b16 a[142:143], v233 offset:45056\n" \
  "v_mfma_f32_16x16x32_bf16 v[8:11], a[128:131], a[104:107], v[8:11]\n" \
  "buffer_load_dwordx4 v240, s[36:39], s49 offen offset:0 lds\n" \
  "v_mfma_f32_16x16x32_bf16 v[12:15], a[128:131], a[108:111], v[12:15]\n" \
  "s_waitcnt lgkmcnt(6)\n" \
  "v_mfma_f32_16x16x32_bf16 v[16:19], a[132:135], a[96:99], v[16:19]\n" \
  "ds_read_b64_tr_b16 a[144:145], v234 offset:32768\n" \
  "v_mfma_f32_16x16x32_bf16 v[20:23], a[132:135], a[100:103], v[20:23]\n" \
  "ds_read_b64_tr_b16 a[146:147], v234 offset:36864\n" \
  "v_mfma_f32_16x16x32_bf16 v[24:27], a[132:135], a[104:107], v[24:27]\n" \
  "buffer_load_dwordx4 a[0:3], v244, s[40:43], s50 offen offset:0 sc1\n" \
  "v_mfma_f32_16x16x32_bf16 v[28:31], a[132:135], a[108:111], v[28:31]\n" \
  "s_waitcnt lgkmcnt(6)\n" \
  "v_mfma_f32_16x16x32_bf16 v[32:35], a[136:139], a[96:99], v[32:35]\n" \
  "ds_read_b64_tr_b16 a[148:149], v235 offset:32768\n" \
  "v_mfma_f32_16x16x32_bf16 v[36:39], a[136:139], a[100:103], v[36:39]\n" \
  "ds_read_b64_tr_b16 a[150:151], v235 offset:36864\n" \
  "v_mfma_f32_16x16x32_bf16 v[40:43], a[136:139], a[104:107], v[40:43]\n" \
  "buffer_load_dwordx4 v241, s[36:39], s49 offen offset:1024 lds\n" \
  "v_mfma_f32_16x16x32_bf16 v[44:47], a[136:139], a[108:111], v[44:47]\n" \
  "s_waitcnt lgkmcnt(6)\n" \
  "v_mfma_f32_16x16x32_bf16 v[192:195], a[176:179], a[96:99], v[192:195]\n" \
  "ds_read_b64_tr_b16 a[180:181], v239 offset:32768\n" \
  "v_mfma_f32_16x16x32_bf16 v[196:199], a[176:179], a[100:103], v[196:199]\n" \
  "ds_read_b64_tr_b16 a[182:183], v239 offset:36864\n" \
  "s_waitcnt lgkmcnt(6)\n" \
  "v_mfma_f32_16x16x32_bf16 v[48:51], a[140:143], a[96:99], v[48:51]\n" \
  "ds_read_b64_tr_b16 a[152:153], v234 offset:40960\n" \
  "buffer_load_dwordx4 a[4:7], v244, s[40:43], s50 offen offset:1024 sc1\n" \
  "v_mfma_f32_16x16x32_bf16 v[52:55], a[140:143], a[100:103], v[52:55]\n" \
  "ds_read_b64_tr_b16 a[154:155], v234 offset:45056\n" \
  "v_mfma_f32_16x16x32_bf16 v[56:59], a[140:143], a[104:107], v[56:59]\n" \
  "v_mfma_f32_16x16x32_bf16 v[60:63], a[140:143], a[108:111], v[60:63]\n" \
  "s_waitcnt lgkmcnt(6)\n" \
  "v_mfma_f32_16x16x32_bf16 v[64:67], a[144:147], a[96:99], v[64:67]\n" \
  "ds_read_b64_tr_b16 a[156:157], v235 offset:40960\n" \
  "buffer_load_dwordx4 v242, s[36:39], s49 offen offset:2048 lds\n" \
  "v_mfma_f32_16x16x32_bf16 v[68:71], a[144:147], a[100:103], v[68:71]\n" \
  "ds_read_b64_tr_b16 a[158:159], v235 offset:45056\n" \
  "v_mfma_f32_16x16x32_bf16 v[72:75], a[144:147], a[104:107], v[72:75]\n" \
  "v_mfma_f32_16x16x32_bf16 v[76:79], a[144:147], a[108:111], v[76:79]\n" \
  "s_waitcnt lgkmcnt(6)\n" \
  "v_mfma_f32_16x16x32_bf16 v[80:83], a[148:151], a[96:99], v[80:83]\n" \
  "ds_read_b64_tr_b16 a[160:161], v236 offset:32768\n" \
  "buffer_load_dwordx4 a[8:11], v245, s[40:43], s50 offen offset:0 sc1\n" \
  "v_mfma_f32_16x16x32_bf16 v[84:87], a[148:151], a[100:103], v[84:87]\n" \
  "ds_read_b64_tr_b16 a[162:163], v236 offset:36864\n" \
  "v_mfma_f32_16x16x32_bf16 v[88:91], a[148:151], a[104:107], v[88:91]\n" \
  "v_mfma_f32_16x16x32_bf16 v[92:95], a[148:151], a[108:111], v[92:95]\n" \
  "s_waitcnt lgkmcnt(6)\n" \
  "v_mfma_f32_16x16x32_bf16 v[200:203], a[180:183], a[96:99], v[200:203]\n" \
  "ds_read_b64_tr_b16 a[184:185], v238 offset:40960\n" \
  "buffer_load_dwordx4 v243, s[36:39], s49 offen offset:3072 lds\n" \
  "v_mfma_f32_16x16x32_bf16 v[204:207], a[180:183], a[100:103], v[204:207]\n" \
  "ds_read_b64_tr_b16 a[186:187], v238 offset:45056\n" \
  "s_waitcnt lgkmcnt(6)\n" \
  "v_mfma_f32_16x16x32_bf16 v[96:99], a[152:155], a[96:99], v[96:99]\n" \
  "ds_read_b64_tr_b16 a[164:165], v237 offset:32768\n" \
  "v_mfma_f32_16x16x32_bf16 v[100:103], a[152:155], a[100:103], v[100:103]\n" \
  "ds_read_b64_tr_b16 a[166:167], v237 offset:36864\n" \
  "v_mfma_f32_16x16x32_bf16 v[104:107], a[152:155], a[104:107], v[104:107]\n" \
  "buffer_load_dwordx4 a[12:15], v245, s[40:43], s50 offen offset:1024 sc1\n" \
  "v_mfma_f32_16x16x32_bf16 v[108:111], a[152:155], a[108:111], v[108:111]\n" \
  "s_add_u32 s49, s49, 229376\n" \
  "s_waitcnt lgkmcnt(6)\n" \
  "v_mfma_f32_16x16x32_bf16 v[112:115], a[156:159], a[96:99], v[112:115]\n" \
  "ds_read_b64_tr_b16 a[168:169], v236 offset:40960\n" \
  "s_add_u32 s50, s50, 4096\n" \
  "v_mfma_f32_16x16x32_bf16 v[116:119], a[156:159], a[100:103], v[116:119]\n" \
  "ds_read_b64_tr_b16 a[170:171], v236 offset:45056\n" \
  "v_mfma_f32_16x16x32_bf16 v[120:123], a[156:159], a[104:107], v[120:123]\n" \
  "v_mfma_f32_16x16x32_bf16 v[124:127], a[156:159], a[108:111], v[124:127]\n" \
  "s_waitcnt lgkmcnt(6)\n" \
  "v_mfma_f32_16x16x32_bf16 v[128:131], a[160:163], a[96:99], v[128:131]\n" \
  "ds_read_b64_tr_b16 a[172:173], v237 offset:40960\n" \
  "v_mfma_f32_16x16x32_bf16 v[132:135], a[160:163], a[100:103], v[132:135]\n" \
  "ds_read_b64_tr_b16 a[174:175], v237 offset:45056\n" \
  "v_mfma_f32_16x16x32_bf16 v[136:139], a[160:163], a[104:107], v[136:139]\n" \
  "v_mfma_f32_16x16x32_bf16 v[140:143], a[160:163], a[108:111], v[140:143]\n" \
  "s_waitcnt lgkmcnt(6)\n" \
  "v_mfma_f32_16x16x32_bf16 v[208:211], a[184:187], a[96:99], v[208:211]\n" \
  "ds_read_b64_tr_b16 a[188:189], v239 offset:40960\n" \
  "v_mfma_f32_16x16x32_bf16 v[212:215], a[184:187], a[100:103], v[212:215]\n" \
  "ds_read_b64_tr_b16 a[190:191], v239 offset:45056\n" \
  "s_waitcnt vmcnt(9)\n" \
  "s_barrier\n" \
  "s_waitcnt lgkmcnt(6)\n" \
  "v_mfma_f32_16x16x32_bf16 v[144:147], a[164:167], a[96:99], v[144:147]\n" \
  "ds_read_b64_tr_b16 a[128:129], v232 offset:49152\n" \
  "v_mfma_f32_16x16x32_bf16 v[148:151], a[164:167], a[100:103], v[148:151]\n" \
  "ds_read_b64_tr_b16 a[130:131], v232 offset:53248\n" \
  "v_mfma_f32_16x16x32_bf16 v[152:155], a[164:167], a[104:107], v[152:155]\n" \
  "v_mfma_f32_16x16x32_bf16 v[156:159], a[164:167], a[108:111], v[156:159]\n" \
  "s_waitcnt lgkmcnt(6)\n" \
  "v_mfma_f32_16x16x32_bf16 v[160:163], a[168:171], a[96:99], v[160:163]\n" \
  "ds_read_b64_tr_b16 a[132:133], v233 offset:49152\n" \
  "buffer_load_dword v247, v248, s[60:63], s79 offen\n" \
  "v_mfma_f32_16x16x32_bf16 v[164:167], a[168:171], a[100:103], v[164:167]\n" \
  "ds_read_b64_tr_b16 a[134:135], v233 offset:53248\n" \
  "v_mfma_f32_16x16x32_bf16 v[168:171], a[168:171], a[104:107], v[168:171]\n" \
  "s_add_u32 s79, s79, 458752\n" \
  "v_mfma_f32_16x16x32_bf16 v[172:175], a[168:171], a[108:111], v[172:175]\n" \
  "s_waitcnt lgkmcnt(6)\n" \
  "v_mfma_f32_16x16x32_bf16 v[176:179], a[172:175], a[96:99], v[176:179]\n" \
  "ds_read_b64_tr_b16 a[136:137], v232 offset:57344\n" \
  "v_mfma_f32_16x16x32_bf16 v[180:183], a[172:175], a[100:103], v[180:183]\n" \
  "ds_read_b64_tr_b16 a[138:139], v232 offset:61440\n" \
  "v_mfma_f32_16x16x32_bf16 v[184:187], a[172:175], a[104:107], v[184:187]\n" \
  "v_mfma_f32_16x16x32_bf16 v[188:191], a[172:175], a[108:111], v[188:191]\n" \
  "s_waitcnt lgkmcnt(6)\n" \
  "v_mfma_f32_16x16x32_bf16 v[216:219], a[188:191], a[96:99], v[216:219]\n" \
  "ds_read_b64_tr_b16 a[176:177], v238 offset:49152\n" \
  "v_mfma_f32_16x16x32_bf16 v[220:223], a[188:191], a[100:103], v[220:223]\n" \
  "ds_read_b64_tr_b16 a[178:179], v238 offset:53248\n" \
  "s_waitcnt lgkmcnt(6)\n" \
  "v_mfma_f32_16x16x32_bf16 v[0:3], a[128:131], a[112:115], v[0:3]\n" \
  "s_add_u32 m0, s48, 16384\n" \
  "ds_read_b64_tr_b16 a[140:141], v233 offset:57344\n" \
  "v_mfma_f32_16x16x32_bf16 v[4:7], a[128:131], a[116:119], v[4:7]\n" \
  "ds_read_b64_tr_b16 a[142:143], v233 offset:61440\n" \
  "v_mfma_f32_16x16x32_bf16 v[8:11], a[128:131], a[120:123], v[8:11]\n" \
  "buffer_load_dwordx4 v240, s[36:39], s49 offen offset:0 lds\n" \
  "v_mfma_f32_16x16x32_bf16 v[12:15], a[128:131], a[124:127], v[12:15]\n" \
  "s_waitcnt lgkmcnt(6)\n" \
  "v_mfma_f32_16x16x32_bf16 v[16:19], a[132:135], a[112:115], v[16:19]\n" \
  "ds_read_b64_tr_b16 a[144:145], v234 offset:49152\n" \
  "v_mfma_f32_16x16x32_bf16 v[20:23], a[132:135], a[116:119], v[20:23]\n" \
  "ds_read_b64_tr_b16 a[146:147], v234 offset:53248\n" \
  "v_mfma_f32_16x16x32_bf16 v[24:27], a[132:135], a[120:123], v[24:27]\n" \
  "buffer_load_dwordx4 a[16:19], v244, s[40:43], s50 offen offset:0 sc1\n" \
  "v_mfma_f32_16x16x32_bf16 v[28:31], a[132:135], a[124:127], v[28:31]\n" \
  "s_waitcnt lgkmcnt(6)\n" \
  "v_mfma_f32_16x16x32_bf16 v[32:35], a[136:139], a[112:115], v[32:35]\n" \
  "ds_read_b64_tr_b16 a[148:149], v235 offset:49152\n" \
  "v_mfma_f32_16x16x32_bf16 v[36:39], a[136:139], a[116:119], v[36:39]\n" \
  "ds_read_b64_tr_b16 a[150:151], v235 offset:53248\n" \
  "v_mfma_f32_16x16x32_bf16 v[40:43], a[136:139], a[120:123], v[40:43]\n" \
  "buffer_load_dwordx4 v241, s[36:39], s49 offen offset:1024 lds\n" \
  "v_mfma_f32_16x16x32_bf16 v[44:47], a[136:139], a[124:127], v[44:47]\n" \
  "s_waitcnt lgkmcnt(6)\n" \
  "v_mfma_f32_16x16x32_bf16 v[192:195], a[176:179], a[112:115], v[192:195]\n" \
  "ds_read_b64_tr_b16 a[180:181], v239 offset:49152\n" \
  "v_mfma_f32_16x16x32_bf16 v[196:199], a[176:179], a[116:119], v[196:199]\n" \
  "ds_read_b64_tr_b16 a[182:183], v239 offset:53248\n" \
  "s_waitcnt lgkmcnt(6)\n" \
  "v_mfma_f32_16x16x32_bf16 v[48:51], a[140:143], a[112:115], v[48:51]\n" \
  "ds_read_b64_tr_b16 a[152:153], v234 offset:57344\n" \
  "buffer_load_dwordx4 a[20:23], v244, s[40:43], s50 offen offset:1024 sc1\n" \
  "v_mfma_f32_16x16x32_bf16 v[52:55], a[140:143], a[116:119], v[52:55]\n" \
  "ds_read_b64_tr_b16 a[154:155], v234 offset:61440\n" \
  "v_mfma_f32_16x16x32_bf16 v[56:59], a[140:143], a[120:123], v[56:59]\n" \
  "v_mfma_f32_16x16x32_bf16 v[60:63], a[140:143], a[124:127], v[60:63]\n" \
  "s_waitcnt lgkmcnt(6)\n" \
  "v_mfma_f32_16x16x32_bf16 v[64:67], a[144:147], a[112:115], v[64:67]\n" \
  "ds_read_b64_tr_b16 a[156:157], v235 offset:57344\n" \
  "buffer_load_dwordx4 v242, s[36:39], s49 offen offset:2048 lds\n" \
  "v_mfma_f32_16x16x32_bf16 v[68:71], a[144:147], a[116:119], v[68:71]\n" \
  "ds_read_b64_tr_b16 a[158:159], v235 offset:61440\n" \
  "v_mfma_f32_16x16x32_bf16 v[72:75], a[144:147], a[120:123], v[72:75]\n" \
  "v_mfma_f32_16x16x32_bf16 v[76:79], a[144:147], a[124:127], v[76:79]\n" \
  "s_waitcnt lgkmcnt(6)\n" \
  "v_mfma_f32_16x16x32_bf16 v[80:83], a[148:151], a[112:115], v[80:83]\n" \
  "ds_read_b64_tr_b16 a[160:161], v236 offset:49152\n" \
  "buffer_load_dwordx4 a[24:27], v245, s[40:43], s50 offen offset:0 sc1\n" \
  "v_mfma_f32_16x16x32_bf16 v[84:87], a[148:151], a[116:119], v[84:87]\n" \
  "ds_read_b64_tr_b16 a[162:163], v236 offset:53248\n" \
  "v_mfma_f32_16x16x32_bf16 v[88:91], a[148:151], a[120:123], v[88:91]\n" \
  "v_mfma_f32_16x16x32_bf16 v[92:95], a[148:151], a[124:127], v[92:95]\n" \
  "s_waitcnt lgkmcnt(6)\n" \
  "v_mfma_f32_16x16x32_bf16 v[200:203], a[180:183], a[112:115], v[200:203]\n" \
  "ds_read_b64_tr_b16 a[184:185], v238 offset:57344\n" \
  "buffer_load_dwordx4 v243, s[36:39], s49 offen offset:3072 lds\n" \
  "v_mfma_f32_16x16x32_bf16 v[204:207], a[180:183], a[116:119], v[204:207]\n" \
  "ds_read_b64_tr_b16 a[186:187], v238 offset:61440\n" \
  "s_waitcnt lgkmcnt(6)\n" \
  "v_mfma_f32_16x16x32_bf16 v[96:99], a[152:155], a[112:115], v[96:99]\n" \
  "ds_read_b64_tr_b16 a[164:165], v237 offset:49152\n" \
  "v_mfma_f32_16x16x32_bf16 v[100:103], a[152:155], a[116:119], v[100:103]\n" \
  "ds_read_b64_tr_b16 a[166:167], v237 offset:53248\n" \
  "v_mfma_f32_16x16x32_bf16 v[104:107], a[152:155], a[120:123], v[104:107]\n" \
  "buffer_load_dwordx4 a[28:31], v245, s[40:43], s50 offen offset:1024 sc1\n" \
  "v_mfma_f32_16x16x32_bf16 v[108:111], a[152:155], a[124:127], v[108:111]\n" \
  "s_add_u32 s49, s49, 229376\n" \
  "s_waitcnt lgkmcnt(6)\n" \
  "v_mfma_f32_16x16x32_bf16 v[112:115], a[156:159], a[112:115], v[112:115]\n" \
  "ds_read_b64_tr_b16 a[168:169], v236 offset:57344\n" \
  "s_add_u32 s50, s50, 4096\n" \
  "v_mfma_f32_16x16x32_bf16 v[116:119], a[156:159], a[116:119], v[116:119]\n" \
  "ds_read_b64_tr_b16 a[170:171], v236 offset:61440\n" \
  "v_mfma_f32_16x16x32_bf16 v[120:123], a[156:159], a[120:123], v[120:123]\n" \
  "v_mfma_f32_16x16x32_bf16 v[124:127], a[156:159], a[124:127], v[124:127]\n" \
  "s_waitcnt lgkmcnt(6)\n" \
  "v_mfma_f32_16x16x32_bf16 v[128:131], a[160:163], a[112:115], v[128:131]\n" \
  "ds_read_b64_tr_b16 a[172:173], v237 offset:57344\n" \
  "v_mfma_f32_16x16x32_bf16 v[132:135], a[160:163], a[116:119], v[132:135]\n" \
  "ds_read_b64_tr_b16 a[174:175], v237 offset:61440\n" \
  "v_mfma_f32_16x16x32_bf16 v[136:139], a[160:163], a[120:123], v[136:139]\n" \
  "v_mfma_f32_16x16x32_bf16 v[140:143], a[160:163], a[124:127], v[140:143]\n" \
  "s_waitcnt lgkmcnt(6)\n" \
  "v_mfma_f32_16x16x32_bf16 v[208:211], a[184:187], a[112:115], v[208:211]\n" \
  "ds_read_b64_tr_b16 a[188:189], v239 offset:57344\n" \
  "v_mfma_f32_16x16x32_bf16 v[212:215], a[184:187], a[116:119], v[212:215]\n" \
  "ds_read_b64_tr_b16 a[190:191], v239 offset:61440\n" \
  "s_waitcnt vmcnt(9)\n" \
  "s_barrier\n" \
  "s_waitcnt lgkmcnt(6)\n" \
  "v_mfma_f32_16x16x32_bf16 v[144:147], a[164:167], a[112:115], v[144:147]\n" \
  "ds_read_b64_tr_b16 a[128:129], v224 offset:0\n" \
  "v_mfma_f32_16x16x32_bf16 v[148:151], a[164:167], a[116:119], v[148:151]\n" \
  "ds_read_b64_tr_b16 a[130:131], v224 offset:4096\n" \
  "v_mfma_f32_16x16x32_bf16 v[152:155], a[164:167], a[120:123], v[152:155]\n" \
  "v_mfma_f32_16x16x32_bf16 v[156:159], a[164:167], a[124:127], v[156:159]\n" \
  "s_waitcnt lgkmcnt(6)\n" \
  "v_mfma_f32_16x16x32_bf16 v[160:163], a[168:171], a[112:115], v[160:163]\n" \
  "ds_read_b64_tr_b16 a[132:133], v225 offset:0\n" \
  "v_mfma_f32_16x16x32_bf16 v[164:167], a[168:171], a[116:119], v[164:167]\n" \
  "ds_read_b64_tr_b16 a[134:135], v225 offset:4096\n" \
  "v_mfma_f32_16x16x32_bf16 v[168:171], a[168:171], a[120:123], v[168:171]\n" \
  "v_mfma_f32_16x16x32_bf16 v[172:175], a[168:171], a[124:127], v[172:175]\n" \
  "s_waitcnt lgkmcnt(6)\n" \
  "v_mfma_f32_16x16x32_bf16 v[176:179], a[172:175], a[112:115], v[176:179]\n" \
  "ds_read_b64_tr_b16 a[136:137], v224 offset:8192\n" \
  "v_mfma_f32_16x16x32_bf16 v[180:183], a[172:175], a[116:119], v[180:183]\n" \
  "ds_read_b64_tr_b16 a[138:139], v224 offset:12288\n" \
  "v_mfma_f32_16x16x32_bf16 v[184:187], a[172:175], a[120:123], v[184:187]\n" \
  "v_mfma_f32_16x16x32_bf16 v[188:191], a[172:175], a[124:127], v[188:191]\n" \
  "s_add_u32 s51, s51, 1\n" \
  "s_waitcnt lgkmcnt(6)\n" \
  "v_mfma_f32_16x16x32_bf16 v[216:219], a[188:191], a[112:115], v[216:219]\n" \
  "ds_read_b64_tr_b16 a[176:177], v230 offset:0\n" \
  "s_cmp_lt_u32 s51, 4\n" \
  "v_mfma_f32_16x16x32_bf16 v[220:223], a[188:191], a[116:119], v[220:223]\n" \
  "ds_read_b64_tr_b16 a[178:179], v230 offset:4096\n" \
  "s_cbranch_scc1 L_G1_loop0_%=\n" \
  "L_G1_done_%=:\n" \
  "s_nop 7\n" \
  "s_nop 7\n" \
  ".Lcb_g1epi:\n" \
  "v_mov_b32 v232, 0xc0135761\n" \
  "v_mov_b32 v233, 0xc0135761\n" \
  "v_mov_b32 v234, 0xbdd2d3e8\n" \
  "v_mov_b32 v235, 0xbdd2d3e8\n" \
  "v_pk_mul_f32 v[224:225], v[0:1], v[0:1]\n" \
  "v_pk_mul_f32 v[226:227], v[2:3], v[2:3]\n" \
  "v_pk_mul_f32 v[228:229], v[16:17], v[16:17]\n" \
  "v_pk_mul_f32 v[230:231], v[18:19], v[18:19]\n" \
  "v_pk_fma_f32 v[224:225], v[224:225], v[234:235], v[232:233]\n" \
  "v_pk_fma_f32 v[226:227], v[226:227], v[234:235], v[232:233]\n" \
  "v_pk_fma_f32 v[228:229], v[228:229], v[234:235], v[232:233]\n" \
  "v_pk_fma_f32 v[230:231], v[230:231], v[234:235], v[232:233]\n" \
  "v_pk_mul_f32 v[224:225], v[224:225], v[0:1]\n" \
  "v_pk_mul_f32 v[226:227], v[226:227], v[2:3]\n" \
  "v_pk_mul_f32 v[228:229], v[228:229], v[16:17]\n" \
  "v_pk_mul_f32 v[230:231], v[230:231], v[18:19]\n" \
  "v_exp_f32 v224, v224\n" \
  "v_exp_f32 v225, v225\n" \
  "v_exp_f32 v226, v226\n" \
  "v_exp_f32 v227, v227\n" \
  "v_exp_f32 v228, v228\n" \
  "v_exp_f32 v229, v229\n" \
  "v_exp_f32 v230, v230\n" \
  "v_exp_f32 v231, v231\n" \
  "v_pk_mul_f32 v[32:33], v[0:1], v[32:33]\n" \
  "v_pk_mul_f32 v[34:35], v[2:3], v[34:35]\n" \
  "v_pk_mul_f32 v[48:49], v[16:17], v[48:49]\n" \
  "v_pk_mul_f32 v[50:51], v[18:19], v[50:51]\n" \
  "v_pk_add_f32 v[224:225], v[224:225], 1.0 op_sel_hi:[1,0]\n" \
  "v_pk_add_f32 v[226:227], v[226:227], 1.0 op_sel_hi:[1,0]\n" \
  "v_pk_add_f32 v[228:229], v[228:229], 1.0 op_sel_hi:[1,0]\n" \
  "v_pk_add_f32 v[230:231], v[230:231], 1.0 op_sel_hi:[1,0]\n" \
  "v_rcp_f32 v224, v224\n" \
  "v_rcp_f32 v225, v225\n" \
  "v_rcp_f32 v226, v226\n" \
  "v_rcp_f32 v227, v227\n" \
  "v_rcp_f32 v228, v228\n" \
  "v_rcp_f32 v229, v229\n" \
  "v_rcp_f32 v230, v230\n" \
  "v_rcp_f32 v231, v231\n" \
  "s_nop 0\n" \
  "v_pk_mul_f32 v[32:33], v[32:33], v[224:225]\n" \
  "v_pk_mul_f32 v[34:35], v[34:35], v[226:227]\n" \
  "v_pk_mul_f32 v[48:49], v[48:49], v[228:229]\n" \
  "v_pk_mul_f32 v[50:51], v[50:51], v[230:231]\n" \
  "v_cvt_pk_bf16_f32 v0, v32, v33\n" \
  "v_cvt_pk_bf16_f32 v1, v34, v35\n" \
  "v_cvt_pk_bf16_f32 v2, v48, v49\n" \
  "v_cvt_pk_bf16_f32 v3, v50, v51\n" \
  "buffer_store_dwordx4 v[0:3], v246, s[44:47], s52 offen offset:0 sc1\n" \
  "v_pk_mul_f32 v[224:225], v[4:5], v[4:5]\n" \
  "v_pk_mul_f32 v[226:227], v[6:7], v[6:7]\n" \
  "v_pk_mul_f32 v[228:229], v[20:21], v[20:21]\n" \
  "v_pk_mul_f32 v[230:231], v[22:23], v[22:23]\n" \
  "v_pk_fma_f32 v[224:225], v[224:225], v[234:235], v[232:233]\n" \
  "v_pk_fma_f32 v[226:227], v[226:227], v[234:235], v[232:233]\n" \
  "v_pk_fma_f32 v[228:229], v[228:229], v[234:235], v[232:233]\n" \
  "v_pk_fma_f32 v[230:231], v[230:231], v[234:235], v[232:233]\n" \
  "v_pk_mul_f32 v[224:225], v[224:225], v[4:5]\n" \
  "v_pk_mul_f32 v[226:227], v[226:227], v[6:7]\n" \
  "v_pk_mul_f32 v[228:229], v[228:229], v[20:21]\n" \
  "v_pk_mul_f32 v[230:231], v[230:231], v[22:23]\n" \
  "v_exp_f32 v224, v224\n" \
  "v_exp_f32 v225, v225\n" \
  "v_exp_f32 v226, v226\n" \
  "v_exp_f32 v227, v227\n" \
  "v_exp_f32 v228, v228\n" \
  "v_exp_f32 v229, v229\n" \
  "v_exp_f32 v230, v230\n" \
  "v_exp_f32 v231, v231\n" \
  "v_pk_mul_f32 v[36:37], v[4:5], v[36:37]\n" \
  "v_pk_mul_f32 v[38:39], v[6:7], v[38:39]\n" \
  "v_pk_mul_f32 v[52:53], v[20:21], v[52:53]\n" \
  "v_pk_mul_f32 v[54:55], v[22:23], v[54:55]\n" \
  "v_pk_add_f32 v[224:225], v[224:225], 1.0 op_sel_hi:[1,0]\n" \
  "v_pk_add_f32 v[226:227], v[226:227], 1.0 op_sel_hi:[1,0]\n" \
  "v_pk_add_f32 v[228:229], v[228:229], 1.0 op_sel_hi:[1,0]\n" \
  "v_pk_add_f32 v[230:231], v[230:231], 1.0 op_sel_hi:[1,0]\n" \
  "v_rcp_f32 v224, v224\n" \
  "v_rcp_f32 v225, v225\n" \
  "v_rcp_f32 v226, v226\n" \
  "v_rcp_f32 v227, v227\n" \
  "v_rcp_f32 v228, v228\n" \
  "v_rcp_f32 v229, v229\n" \
  "v_rcp_f32 v230, v230\n" \
  "v_rcp_f32 v231, v231\n" \
  "s_nop 0\n" \
  "v_pk_mul_f32 v[36:37], v[36:37], v[224:225]\n" \
  "v_pk_mul_f32 v[38:39], v[38:39], v[226:227]\n" \
  "v_pk_mul_f32 v[52:53], v[52:53], v[228:229]\n" \
  "v_pk_mul_f32 v[54:55], v[54:55], v[230:231]\n" \
  "v_cvt_pk_bf16_f32 v4, v36, v37\n" \
  "v_cvt_pk_bf16_f32 v5, v38, v39\n" \
  "v_cvt_pk_bf16_f32 v6, v52, v53\n" \
  "v_cvt_pk_bf16_f32 v7, v54, v55\n" \
  "buffer_store_dwordx4 v[4:7], v246, s[44:47], s52 offen offset:1024 sc1\n" \
  "v_pk_mul_f32 v[224:225], v[8:9], v[8:9]\n" \
  "v_pk_mul_f32 v[226:227], v[10:11], v[10:11]\n" \
  "v_pk_mul_f32 v[228:229], v[24:25], v[24:25]\n" \
  "v_pk_mul_f32 v[230:231], v[26:27], v[26:27]\n" \
  "v_pk_fma_f32 v[224:225], v[224:225], v[234:235], v[232:233]\n" \
  "v_pk_fma_f32 v[226:227], v[226:227], v[234:235], v[232:233]\n" \
  "v_pk_fma_f32 v[228:229], v[228:229], v[234:235], v[232:233]\n" \
  "v_pk_fma_f32 v[230:231], v[230:231], v[234:235], v[232:233]\n" \
  "v_pk_mul_f32 v[224:225], v[224:225], v[8:9]\n" \
  "v_pk_mul_f32 v[226:227], v[226:227], v[10:11]\n" \
  "v_pk_mul_f32 v[228:229], v[228:229], v[24:25]\n" \
  "v_pk_mul_f32 v[230:231], v[230:231], v[26:27]\n" \
  "v_exp_f32 v224, v224\n" \
  "v_exp_f32 v225, v225\n" \
  "v_exp_f32 v226, v226\n" \
  "v_exp_f32 v227, v227\n" \
  "v_exp_f32 v228, v228\n" \
  "v_exp_f32 v229, v229\n" \
  "v_exp_f32 v230, v230\n" \
  "v_exp_f32 v231, v231\n" \
  "v_pk_mul_f32 v[40:41], v[8:9], v[40:41]\n" \
  "v_pk_mul_f32 v[42:43], v[10:11], v[42:43]\n" \
  "v_pk_mul_f32 v[56:57], v[24:25], v[56:57]\n" \
  "v_pk_mul_f32 v[58:59], v[26:27], v[58:59]\n" \
  "v_pk_add_f32 v[224:225], v[224:225], 1.0 op_sel_hi:[1,0]\n" \
  "v_pk_add_f32 v[226:227], v[226:227], 1.0 op_sel_hi:[1,0]\n" \
  "v_pk_add_f32 v[228:229], v[228:229], 1.0 op_sel_hi:[1,0]\n" \
  "v_pk_add_f32 v[230:231], v[230:231], 1.0 op_sel_hi:[1,0]\n" \
  "v_rcp_f32 v224, v224\n" \
  "v_rcp_f32 v225, v225\n" \
  "v_rcp_f32 v226, v226\n" \
  "v_rcp_f32 v227, v227\n" \
  "v_rcp_f32 v228, v228\n" \
  "v_rcp_f32 v229, v229\n" \
  "v_rcp_f32 v230, v230\n" \
  "v_rcp_f32 v231, v231\n" \
  "s_nop 0\n" \
  "v_pk_mul_f32 v[40:41], v[40:41], v[224:225]\n" \
  "v_pk_mul_f32 v[42:43], v[42:43], v[226:227]\n" \
  "v_pk_mul_f32 v[56:57], v[56:57], v[228:229]\n" \
  "v_pk_mul_f32 v[58:59], v[58:59], v[230:231]\n" \
  "v_cvt_pk_bf16_f32 v8, v40, v41\n" \
  "v_cvt_pk_bf16_f32 v9, v42, v43\n" \
  "v_cvt_pk_bf16_f32 v10, v56, v57\n" \
  "v_cvt_pk_bf16_f32 v11, v58, v59\n" \
  "buffer_store_dwordx4 v[8:11], v246, s[44:47], s53 offen offset:0 sc1\n" \
  "v_pk_mul_f32 v[224:225], v[12:13], v[12:13]\n" \
  "v_pk_mul_f32 v[226:227], v[14:15], v[14:15]\n" \
  "v_pk_mul_f32 v[228:229], v[28:29], v[28:29]\n" \
  "v_pk_mul_f32 v[230:231], v[30:31], v[30:31]\n" \
  "v_pk_fma_f32 v[224:225], v[224:225], v[234:235], v[232:233]\n" \
  "v_pk_fma_f32 v[226:227], v[226:227], v[234:235], v[232:233]\n" \
  "v_pk_fma_f32 v[228:229], v[228:229], v[234:235], v[232:233]\n" \
  "v_pk_fma_f32 v[230:231], v[230:231], v[234:235], v[232:233]\n" \
  "v_pk_mul_f32 v[224:225], v[224:225], v[12:13]\n" \
  "v_pk_mul_f32 v[226:227], v[226:227], v[14:15]\n" \
  "v_pk_mul_f32 v[228:229], v[228:229], v[28:29]\n" \
  "v_pk_mul_f32 v[230:231], v[230:231], v[30:31]\n" \
  "v_exp_f32 v224, v224\n" \
  "v_exp_f32 v225, v225\n" \
  "v_exp_f32 v226, v226\n" \
  "v_exp_f32 v227, v227\n" \
  "v_exp_f32 v228, v228\n" \
  "v_exp_f32 v229, v229\n" \
  "v_exp_f32 v230, v230\n" \
  "v_exp_f32 v231, v231\n" \
  "v_pk_mul_f32 v[44:45], v[12:13], v[44:45]\n" \
  "v_pk_mul_f32 v[46:47], v[14:15], v[46:47]\n" \
  "v_pk_mul_f32 v[60:61], v[28:29], v[60:61]\n" \
  "v_pk_mul_f32 v[62:63], v[30:31], v[62:63]\n" \
  "v_pk_add_f32 v[224:225], v[224:225], 1.0 op_sel_hi:[1,0]\n" \
  "v_pk_add_f32 v[226:227], v[226:227], 1.0 op_sel_hi:[1,0]\n" \
  "v_pk_add_f32 v[228:229], v[228:229], 1.0 op_sel_hi:[1,0]\n" \
  "v_pk_add_f32 v[230:231], v[230:231], 1.0 op_sel_hi:[1,0]\n" \
  "v_rcp_f32 v224, v224\n" \
  "v_rcp_f32 v225, v225\n" \
  "v_rcp_f32 v226, v226\n" \
  "v_rcp_f32 v227, v227\n" \
  "v_rcp_f32 v228, v228\n" \
  "v_rcp_f32 v229, v229\n" \
  "v_rcp_f32 v230, v230\n" \
  "v_rcp_f32 v231, v231\n" \
  "s_nop 0\n" \
  "v_pk_mul_f32 v[44:45], v[44:45], v[224:225]\n" \
  "v_pk_mul_f32 v[46:47], v[46:47], v[226:227]\n" \
  "v_pk_mul_f32 v[60:61], v[60:61], v[228:229]\n" \
  "v_pk_mul_f32 v[62:63], v[62:63], v[230:231]\n" \
  "v_cvt_pk_bf16_f32 v12, v44, v45\n" \
  "v_cvt_pk_bf16_f32 v13, v46, v47\n" \
  "v_cvt_pk_bf16_f32 v14, v60, v61\n" \
  "v_cvt_pk_bf16_f32 v15, v62, v63\n" \
  "buffer_store_dwordx4 v[12:15], v246, s[44:47], s53 offen offset:1024 sc1\n" \
  "v_pk_mul_f32 v[224:225], v[64:65], v[64:65]\n" \
  "v_pk_mul_f32 v[226:227], v[66:67], v[66:67]\n" \
  "v_pk_mul_f32 v[228:229], v[80:81], v[80:81]\n" \
  "v_pk_mul_f32 v[230:231], v[82:83], v[82:83]\n" \
  "v_pk_fma_f32 v[224:225], v[224:225], v[234:235], v[232:233]\n" \
  "v_pk_fma_f32 v[226:227], v[226:227], v[234:235], v[232:233]\n" \
  "v_pk_fma_f32 v[228:229], v[228:229], v[234:235], v[232:233]\n" \
  "v_pk_fma_f32 v[230:231], v[230:231], v[234:235], v[232:233]\n" \
  "v_pk_mul_f32 v[224:225], v[224:225], v[64:65]\n" \
  "v_pk_mul_f32 v[226:227], v[226:227], v[66:67]\n" \
  "v_pk_mul_f32 v[228:229], v[228:229], v[80:81]\n" \
  "v_pk_mul_f32 v[230:231], v[230:231], v[82:83]\n" \
  "v_exp_f32 v224, v224\n" \
  "v_exp_f32 v225, v225\n" \
  "v_exp_f32 v226, v226\n" \
  "v_exp_f32 v227, v227\n" \
  "v_exp_f32 v228, v228\n" \
  "v_exp_f32 v229, v229\n" \
  "v_exp_f32 v230, v230\n" \
  "v_exp_f32 v231, v231\n" \
  "v_pk_mul_f32 v[96:97], v[64:65], v[96:97]\n" \
  "v_pk_mul_f32 v[98:99], v[66:67], v[98:99]\n" \
  "v_pk_mul_f32 v[112:113], v[80:81], v[112:113]\n" \
  "v_pk_mul_f32 v[114:115], v[82:83], v[114:115]\n" \
  "v_pk_add_f32 v[224:225], v[224:225], 1.0 op_sel_hi:[1,0]\n" \
  "v_pk_add_f32 v[226:227], v[226:227], 1.0 op_sel_hi:[1,0]\n" \
  "v_pk_add_f32 v[228:229], v[228:229], 1.0 op_sel_hi:[1,0]\n" \
  "v_pk_add_f32 v[230:231], v[230:231], 1.0 op_sel_hi:[1,0]\n" \
  "v_rcp_f32 v224, v224\n" \
  "v_rcp_f32 v225, v225\n" \
  "v_rcp_f32 v226, v226\n" \
  "v_rcp_f32 v227, v227\n" \
  "v_rcp_f32 v228, v228\n" \
  "v_rcp_f32 v229, v229\n" \
  "v_rcp_f32 v230, v230\n" \
  "v_rcp_f32 v231, v231\n" \
  "s_nop 0\n" \
  "v_pk_mul_f32 v[96:97], v[96:97], v[224:225]\n" \
  "v_pk_mul_f32 v[98:99], v[98:99], v[226:227]\n" \
  "v_pk_mul_f32 v[112:113], v[112:113], v[228:229]\n" \
  "v_pk_mul_f32 v[114:115], v[114:115], v[230:231]\n" \
  "v_cvt_pk_bf16_f32 v64, v96, v97\n" \
  "v_cvt_pk_bf16_f32 v65, v98, v99\n" \
  "v_cvt_pk_bf16_f32 v66, v112, v113\n" \
  "v_cvt_pk_bf16_f32 v67, v114, v115\n" \
  "buffer_store_dwordx4 v[64:67], v246, s[44:47], s54 offen offset:0 sc1\n" \
  "v_pk_mul_f32 v[224:225], v[68:69], v[68:69]\n" \
  "v_pk_mul_f32 v[226:227], v[70:71], v[70:71]\n" \
  "v_pk_mul_f32 v[228:229], v[84:85], v[84:85]\n" \
  "v_pk_mul_f32 v[230:231], v[86:87], v[86:87]\n" \
  "v_pk_fma_f32 v[224:225], v[224:225], v[234:235], v[232:233]\n" \
  "v_pk_fma_f32 v[226:227], v[226:227], v[234:235], v[232:233]\n" \
  "v_pk_fma_f32 v[228:229], v[228:229], v[234:235], v[232:233]\n" \
  "v_pk_fma_f32 v[230:231], v[230:231], v[234:235], v[232:233]\n" \
  "v_pk_mul_f32 v[224:225], v[224:225], v[68:69]\n" \
  "v_pk_mul_f32 v[226:227], v[226:227], v[70:71]\n" \
  "v_pk_mul_f32 v[228:229], v[228:229], v[84:85]\n" \
  "v_pk_mul_f32 v[230:231], v[230:231], v[86:87]\n" \
  "v_exp_f32 v224, v224\n" \
  "v_exp_f32 v225, v225\n" \
  "v_exp_f32 v226, v226\n" \
  "v_exp_f32 v227, v227\n" \
  "v_exp_f32 v228, v228\n" \
  "v_exp_f32 v229, v229\n" \
  "v_exp_f32 v230, v230\n" \
  "v_exp_f32 v231, v231\n" \
  "v_pk_mul_f32 v[100:101], v[68:69], v[100:101]\n" \
  "v_pk_mul_f32 v[102:103], v[70:71], v[102:103]\n" \
  "v_pk_mul_f32 v[116:117], v[84:85], v[116:117]\n" \
  "v_pk_mul_f32 v[118:119], v[86:87], v[118:119]\n" \
  "v_pk_add_f32 v[224:225], v[224:225], 1.0 op_sel_hi:[1,0]\n" \
  "v_pk_add_f32 v[226:227], v[226:227], 1.0 op_sel_hi:[1,0]\n" \
  "v_pk_add_f32 v[228:229], v[228:229], 1.0 op_sel_hi:[1,0]\n" \
  "v_pk_add_f32 v[230:231], v[230:231], 1.0 op_sel_hi:[1,0]\n" \
  "v_rcp_f32 v224, v224\n" \
  "v_rcp_f32 v225, v225\n" \
  "v_rcp_f32 v226, v226\n" \
  "v_rcp_f32 v227, v227\n" \
  "v_rcp_f32 v228, v228\n" \
  "v_rcp_f32 v229, v229\n" \
  "v_rcp_f32 v230, v230\n" \
  "v_rcp_f32 v231, v231\n" \
  "s_nop 0\n" \
  "v_pk_mul_f32 v[100:101], v[100:101], v[224:225]\n" \
  "v_pk_mul_f32 v[102:103], v[102:103], v[226:227]\n" \
  "v_pk_mul_f32 v[116:117], v[116:117], v[228:229]\n" \
  "v_pk_mul_f32 v[118:119], v[118:119], v[230:231]\n" \
  "v_cvt_pk_bf16_f32 v68, v100, v101\n" \
  "v_cvt_pk_bf16_f32 v69, v102, v103\n" \
  "v_cvt_pk_bf16_f32 v70, v116, v117\n" \
  "v_cvt_pk_bf16_f32 v71, v118, v119\n" \
  "buffer_store_dwordx4 v[68:71], v246, s[44:47], s54 offen offset:1024 sc1\n" \
  "v_pk_mul_f32 v[224:225], v[72:73], v[72:73]\n" \
  "v_pk_mul_f32 v[226:227], v[74:75], v[74:75]\n" \
  "v_pk_mul_f32 v[228:229], v[88:89], v[88:89]\n" \
  "v_pk_mul_f32 v[230:231], v[90:91], v[90:91]\n" \
  "v_pk_fma_f32 v[224:225], v[224:225], v[234:235], v[232:233]\n" \
  "v_pk_fma_f32 v[226:227], v[226:227], v[234:235], v[232:233]\n" \
  "v_pk_fma_f32 v[228:229], v[228:229], v[234:235], v[232:233]\n" \
  "v_pk_fma_f32 v[230:231], v[230:231], v[234:235], v[232:233]\n" \
  "v_pk_mul_f32 v[224:225], v[224:225], v[72:73]\n" \
  "v_pk_mul_f32 v[226:227], v[226:227], v[74:75]\n" \
  "v_pk_mul_f32 v[228:229], v[228:229], v[88:89]\n" \
  "v_pk_mul_f32 v[230:231], v[230:231], v[90:91]\n" \
  "v_exp_f32 v224, v224\n" \
  "v_exp_f32 v225, v225\n" \
  "v_exp_f32 v226, v226\n" \
  "v_exp_f32 v227, v227\n" \
  "v_exp_f32 v228, v228\n" \
  "v_exp_f32 v229, v229\n" \
  "v_exp_f32 v230, v230\n" \
  "v_exp_f32 v231, v231\n" \
  "v_pk_mul_f32 v[104:105], v[72:73], v[104:105]\n" \
  "v_pk_mul_f32 v[106:107], v[74:75], v[106:107]\n" \
  "v_pk_mul_f32 v[120:121], v[88:89], v[120:121]\n" \
  "v_pk_mul_f32 v[122:123], v[90:91], v[122:123]\n" \
  "v_pk_add_f32 v[224:225], v[224:225], 1.0 op_sel_hi:[1,0]\n" \
  "v_pk_add_f32 v[226:227], v[226:227], 1.0 op_sel_hi:[1,0]\n" \
  "v_pk_add_f32 v[228:229], v[228:229], 1.0 op_sel_hi:[1,0]\n" \
  "v_pk_add_f32 v[230:231], v[230:231], 1.0 op_sel_hi:[1,0]\n" \
  "v_rcp_f32 v224, v224\n" \
  "v_rcp_f32 v225, v225\n" \
  "v_rcp_f32 v226, v226\n" \
  "v_rcp_f32 v227, v227\n" \
  "v_rcp_f32 v228, v228\n" \
  "v_rcp_f32 v229, v229\n" \
  "v_rcp_f32 v230, v230\n" \
  "v_rcp_f32 v231, v231\n" \
  "s_nop 0\n" \
  "v_pk_mul_f32 v[104:105], v[104:105], v[224:225]\n" \
  "v_pk_mul_f32 v[106:107], v[106:107], v[226:227]\n" \
  "v_pk_mul_f32 v[120:121], v[120:121], v[228:229]\n" \
  "v_pk_mul_f32 v[122:123], v[122:123], v[230:231]\n" \
  "v_cvt_pk_bf16_f32 v72, v104, v105\n" \
  "v_cvt_pk_bf16_f32 v73, v106, v107\n" \
  "v_cvt_pk_bf16_f32 v74, v120, v121\n" \
  "v_cvt_pk_bf16_f32 v75, v122, v123\n" \
  "buffer_store_dwordx4 v[72:75], v246, s[44:47], s55 offen offset:0 sc1\n" \
  "v_pk_mul_f32 v[224:225], v[76:77], v[76:77]\n" \
  "v_pk_mul_f32 v[226:227], v[78:79], v[78:79]\n" \
  "v_pk_mul_f32 v[228:229], v[92:93], v[92:93]\n" \
  "v_pk_mul_f32 v[230:231], v[94:95], v[94:95]\n" \
  "v_pk_fma_f32 v[224:225], v[224:225], v[234:235], v[232:233]\n" \
  "v_pk_fma_f32 v[226:227], v[226:227], v[234:235], v[232:233]\n" \
  "v_pk_fma_f32 v[228:229], v[228:229], v[234:235], v[232:233]\n" \
  "v_pk_fma_f32 v[230:231], v[230:231], v[234:235], v[232:233]\n" \
  "v_pk_mul_f32 v[224:225], v[224:225], v[76:77]\n" \
  "v_pk_mul_f32 v[226:227], v[226:227], v[78:79]\n" \
  "v_pk_mul_f32 v[228:229], v[228:229], v[92:93]\n" \
  "v_pk_mul_f32 v[230:231], v[230:231], v[94:95]\n" \
  "v_exp_f32 v224, v224\n" \
  "v_exp_f32 v225, v225\n" \
  "v_exp_f32 v226, v226\n" \
  "v_exp_f32 v227, v227\n" \
  "v_exp_f32 v228, v228\n" \
  "v_exp_f32 v229, v229\n" \
  "v_exp_f32 v230, v230\n" \
  "v_exp_f32 v231, v231\n" \
  "v_pk_mul_f32 v[108:109], v[76:77], v[108:109]\n" \
  "v_pk_mul_f32 v[110:111], v[78:79], v[110:111]\n" \
  "v_pk_mul_f32 v[124:125], v[92:93], v[124:125]\n" \
  "v_pk_mul_f32 v[126:127], v[94:95], v[126:127]\n" \
  "v_pk_add_f32 v[224:225], v[224:225], 1.0 op_sel_hi:[1,0]\n" \
  "v_pk_add_f32 v[226:227], v[226:227], 1.0 op_sel_hi:[1,0]\n" \
  "v_pk_add_f32 v[228:229], v[228:229], 1.0 op_sel_hi:[1,0]\n" \
  "v_pk_add_f32 v[230:231], v[230:231], 1.0 op_sel_hi:[1,0]\n" \
  "v_rcp_f32 v224, v224\n" \
  "v_rcp_f32 v225, v225\n" \
  "v_rcp_f32 v226, v226\n" \
  "v_rcp_f32 v227, v227\n" \
  "v_rcp_f32 v228, v228\n" \
  "v_rcp_f32 v229, v229\n" \
  "v_rcp_f32 v230, v230\n" \
  "v_rcp_f32 v231, v231\n" \
  "s_nop 0\n" \
  "v_pk_mul_f32 v[108:109], v[108:109], v[224:225]\n" \
  "v_pk_mul_f32 v[110:111], v[110:111], v[226:227]\n" \
  "v_pk_mul_f32 v[124:125], v[124:125], v[228:229]\n" \
  "v_pk_mul_f32 v[126:127], v[126:127], v[230:231]\n" \
  "v_cvt_pk_bf16_f32 v76, v108, v109\n" \
  "v_cvt_pk_bf16_f32 v77, v110, v111\n" \
  "v_cvt_pk_bf16_f32 v78, v124, v125\n" \
  "v_cvt_pk_bf16_f32 v79, v126, v127\n" \
  "buffer_store_dwordx4 v[76:79], v246, s[44:47], s55 offen offset:1024 sc1\n" \
  "v_pk_mul_f32 v[224:225], v[128:129], v[128:129]\n" \
  "v_pk_mul_f32 v[226:227], v[130:131], v[130:131]\n" \
  "v_pk_mul_f32 v[228:229], v[144:145], v[144:145]\n" \
  "v_pk_mul_f32 v[230:231], v[146:147], v[146:147]\n" \
  "v_pk_fma_f32 v[224:225], v[224:225], v[234:235], v[232:233]\n" \
  "v_pk_fma_f32 v[226:227], v[226:227], v[234:235], v[232:233]\n" \
  "v_pk_fma_f32 v[228:229], v[228:229], v[234:235], v[232:233]\n" \
  "v_pk_fma_f32 v[230:231], v[230:231], v[234:235], v[232:233]\n" \
  "v_pk_mul_f32 v[224:225], v[224:225], v[128:129]\n" \
  "v_pk_mul_f32 v[226:227], v[226:227], v[130:131]\n" \
  "v_pk_mul_f32 v[228:229], v[228:229], v[144:145]\n" \
  "v_pk_mul_f32 v[230:231], v[230:231], v[146:147]\n" \
  "v_exp_f32 v224, v224\n" \
  "v_exp_f32 v225, v225\n" \
  "v_exp_f32 v226, v226\n" \
  "v_exp_f32 v227, v227\n" \
  "v_exp_f32 v228, v228\n" \
  "v_exp_f32 v229, v229\n" \
  "v_exp_f32 v230, v230\n" \
  "v_exp_f32 v231, v231\n" \
  "v_pk_mul_f32 v[160:161], v[128:129], v[160:161]\n" \
  "v_pk_mul_f32 v[162:163], v[130:131], v[162:163]\n" \
  "v_pk_mul_f32 v[176:177], v[144:145], v[176:177]\n" \
  "v_pk_mul_f32 v[178:179], v[146:147], v[178:179]\n" \
  "v_pk_add_f32 v[224:225], v[224:225], 1.0 op_sel_hi:[1,0]\n" \
  "v_pk_add_f32 v[226:227], v[226:227], 1.0 op_sel_hi:[1,0]\n" \
  "v_pk_add_f32 v[228:229], v[228:229], 1.0 op_sel_hi:[1,0]\n" \
  "v_pk_add_f32 v[230:231], v[230:231], 1.0 op_sel_hi:[1,0]\n" \
  "v_rcp_f32 v224, v224\n" \
  "v_rcp_f32 v225, v225\n" \
  "v_rcp_f32 v226, v226\n" \
  "v_rcp_f32 v227, v227\n" \
  "v_rcp_f32 v228, v228\n" \
  "v_rcp_f32 v229, v229\n" \
  "v_rcp_f32 v230, v230\n" \
  "v_rcp_f32 v231, v231\n" \
  "s_nop 0\n" \
  "v_pk_mul_f32 v[160:161], v[160:161], v[224:225]\n" \
  "v_pk_mul_f32 v[162:163], v[162:163], v[226:227]\n" \
  "v_pk_mul_f32 v[176:177], v[176:177], v[228:229]\n" \
  "v_pk_mul_f32 v[178:179], v[178:179], v[230:231]\n" \
  "v_cvt_pk_bf16_f32 v128, v160, v161\n" \
  "v_cvt_pk_bf16_f32 v129, v162, v163\n" \
  "v_cvt_pk_bf16_f32 v130, v176, v177\n" \
  "v_cvt_pk_bf16_f32 v131, v178, v179\n" \
  "buffer_store_dwordx4 v[128:131], v246, s[44:47], s56 offen offset:0 sc1\n" \
  "v_pk_mul_f32 v[224:225], v[132:133], v[132:133]\n" \
  "v_pk_mul_f32 v[226:227], v[134:135], v[134:135]\n" \
  "v_pk_mul_f32 v[228:229], v[148:149], v[148:149]\n" \
  "v_pk_mul_f32 v[230:231], v[150:151], v[150:151]\n" \
  "v_pk_fma_f32 v[224:225], v[224:225], v[234:235], v[232:233]\n" \
  "v_pk_fma_f32 v[226:227], v[226:227], v[234:235], v[232:233]\n" \
  "v_pk_fma_f32 v[228:229], v[228:229], v[234:235], v[232:233]\n" \
  "v_pk_fma_f32 v[230:231], v[230:231], v[234:235], v[232:233]\n" \
  "v_pk_mul_f32 v[224:225], v[224:225], v[132:133]\n" \
  "v_pk_mul_f32 v[226:227], v[226:227], v[134:135]\n" \
  "v_pk_mul_f32 v[228:229], v[228:229], v[148:149]\n" \
  "v_pk_mul_f32 v[230:231], v[230:231], v[150:151]\n" \
  "v_exp_f32 v224, v224\n" \
  "v_exp_f32 v225, v225\n" \
  "v_exp_f32 v226, v226\n" \
  "v_exp_f32 v227, v227\n" \
  "v_exp_f32 v228, v228\n" \
  "v_exp_f32 v229, v229\n" \
  "v_exp_f32 v230, v230\n" \
  "v_exp_f32 v231, v231\n" \
  "v_pk_mul_f32 v[164:165], v[132:133], v[164:165]\n" \
  "v_pk_mul_f32 v[166:167], v[134:135], v[166:167]\n" \
  "v_pk_mul_f32 v[180:181], v[148:149], v[180:181]\n" \
  "v_pk_mul_f32 v[182:183], v[150:151], v[182:183]\n" \
  "v_pk_add_f32 v[224:225], v[224:225], 1.0 op_sel_hi:[1,0]\n" \
  "v_pk_add_f32 v[226:227], v[226:227], 1.0 op_sel_hi:[1,0]\n" \
  "v_pk_add_f32 v[228:229], v[228:229], 1.0 op_sel_hi:[1,0]\n" \
  "v_pk_add_f32 v[230:231], v[230:231], 1.0 op_sel_hi:[1,0]\n" \
  "v_rcp_f32 v224, v224\n" \
  "v_rcp_f32 v225, v225\n" \
  "v_rcp_f32 v226, v226\n" \
  "v_rcp_f32 v227, v227\n" \
  "v_rcp_f32 v228, v228\n" \
  "v_rcp_f32 v229, v229\n" \
  "v_rcp_f32 v230, v230\n" \
  "v_rcp_f32 v231, v231\n" \
  "s_nop 0\n" \
  "v_pk_mul_f32 v[164:165], v[164:165], v[224:225]\n" \
  "v_pk_mul_f32 v[166:167], v[166:167], v[226:227]\n" \
  "v_pk_mul_f32 v[180:181], v[180:181], v[228:229]\n" \
  "v_pk_mul_f32 v[182:183], v[182:183], v[230:231]\n" \
  "v_cvt_pk_bf16_f32 v132, v164, v165\n" \
  "v_cvt_pk_bf16_f32 v133, v166, v167\n" \
  "v_cvt_pk_bf16_f32 v134, v180, v181\n" \
  "v_cvt_pk_bf16_f32 v135, v182, v183\n" \
  "buffer_store_dwordx4 v[132:135], v246, s[44:47], s56 offen offset:1024 sc1\n" \
  "v_pk_mul_f32 v[224:225], v[136:137], v[136:137]\n" \
  "v_pk_mul_f32 v[226:227], v[138:139], v[138:139]\n" \
  "v_pk_mul_f32 v[228:229], v[152:153], v[152:153]\n" \
  "v_pk_mul_f32 v[230:231], v[154:155], v[154:155]\n" \
  "v_pk_fma_f32 v[224:225], v[224:225], v[234:235], v[232:233]\n" \
  "v_pk_fma_f32 v[226:227], v[226:227], v[234:235], v[232:233]\n" \
  "v_pk_fma_f32 v[228:229], v[228:229], v[234:235], v[232:233]\n" \
  "v_pk_fma_f32 v[230:231], v[230:231], v[234:235], v[232:233]\n" \
  "v_pk_mul_f32 v[224:225], v[224:225], v[136:137]\n" \
  "v_pk_mul_f32 v[226:227], v[226:227], v[138:139]\n" \
  "v_pk_mul_f32 v[228:229], v[228:229], v[152:153]\n" \
  "v_pk_mul_f32 v[230:231], v[230:231], v[154:155]\n" \
  "v_exp_f32 v224, v224\n" \
  "v_exp_f32 v225, v225\n" \
  "v_exp_f32 v226, v226\n" \
  "v_exp_f32 v227, v227\n" \
  "v_exp_f32 v228, v228\n" \
  "v_exp_f32 v229, v229\n" \
  "v_exp_f32 v230, v230\n" \
  "v_exp_f32 v231, v231\n" \
  "v_pk_mul_f32 v[168:169], v[136:137], v[168:169]\n" \
  "v_pk_mul_f32 v[170:171], v[138:139], v[170:171]\n" \
  "v_pk_mul_f32 v[184:185], v[152:153], v[184:185]\n" \
  "v_pk_mul_f32 v[186:187], v[154:155], v[186:187]\n" \
  "v_pk_add_f32 v[224:225], v[224:225], 1.0 op_sel_hi:[1,0]\n" \
  "v_pk_add_f32 v[226:227], v[226:227], 1.0 op_sel_hi:[1,0]\n" \
  "v_pk_add_f32 v[228:229], v[228:229], 1.0 op_sel_hi:[1,0]\n" \
  "v_pk_add_f32 v[230:231], v[230:231], 1.0 op_sel_hi:[1,0]\n" \
  "v_rcp_f32 v224, v224\n" \
  "v_rcp_f32 v225, v225\n" \
  "v_rcp_f32 v226, v226\n" \
  "v_rcp_f32 v227, v227\n" \
  "v_rcp_f32 v228, v228\n" \
  "v_rcp_f32 v229, v229\n" \
  "v_rcp_f32 v230, v230\n" \
  "v_rcp_f32 v231, v231\n" \
  "s_nop 0\n" \
  "v_pk_mul_f32 v[168:169], v[168:169], v[224:225]\n" \
  "v_pk_mul_f32 v[170:171], v[170:171], v[226:227]\n" \
  "v_pk_mul_f32 v[184:185], v[184:185], v[228:229]\n" \
  "v_pk_mul_f32 v[186:187], v[186:187], v[230:231]\n" \
  "v_cvt_pk_bf16_f32 v136, v168, v169\n" \
  "v_cvt_pk_bf16_f32 v137, v170, v171\n" \
  "v_cvt_pk_bf16_f32 v138, v184, v185\n" \
  "v_cvt_pk_bf16_f32 v139, v186, v187\n" \
  "buffer_store_dwordx4 v[136:139], v246, s[44:47], s57 offen offset:0 sc1\n" \
  "v_pk_mul_f32 v[224:225], v[140:141], v[140:141]\n" \
  "v_pk_mul_f32 v[226:227], v[142:143], v[142:143]\n" \
  "v_pk_mul_f32 v[228:229], v[156:157], v[156:157]\n" \
  "v_pk_mul_f32 v[230:231], v[158:159], v[158:159]\n" \
  "v_pk_fma_f32 v[224:225], v[224:225], v[234:235], v[232:233]\n" \
  "v_pk_fma_f32 v[226:227], v[226:227], v[234:235], v[232:233]\n" \
  "v_pk_fma_f32 v[228:229], v[228:229], v[234:235], v[232:233]\n" \
  "v_pk_fma_f32 v[230:231], v[230:231], v[234:235], v[232:233]\n" \
  "v_pk_mul_f32 v[224:225], v[224:225], v[140:141]\n" \
  "v_pk_mul_f32 v[226:227], v[226:227], v[142:143]\n" \
  "v_pk_mul_f32 v[228:229], v[228:229], v[156:157]\n" \
  "v_pk_mul_f32 v[230:231], v[230:231], v[158:159]\n" \
  "v_exp_f32 v224, v224\n" \
  "v_exp_f32 v225, v225\n" \
  "v_exp_f32 v226, v226\n" \
  "v_exp_f32 v227, v227\n" \
  "v_exp_f32 v228, v228\n" \
  "v_exp_f32 v229, v229\n" \
  "v_exp_f32 v230, v230\n" \
  "v_exp_f32 v231, v231\n" \
  "v_pk_mul_f32 v[172:173], v[140:141], v[172:173]\n" \
  "v_pk_mul_f32 v[174:175], v[142:143], v[174:175]\n" \
  "v_pk_mul_f32 v[188:189], v[156:157], v[188:189]\n" \
  "v_pk_mul_f32 v[190:191], v[158:159], v[190:191]\n" \
  "v_pk_add_f32 v[224:225], v[224:225], 1.0 op_sel_hi:[1,0]\n" \
  "v_pk_add_f32 v[226:227], v[226:227], 1.0 op_sel_hi:[1,0]\n" \
  "v_pk_add_f32 v[228:229], v[228:229], 1.0 op_sel_hi:[1,0]\n" \
  "v_pk_add_f32 v[230:231], v[230:231], 1.0 op_sel_hi:[1,0]\n" \
  "v_rcp_f32 v224, v224\n" \
  "v_rcp_f32 v225, v225\n" \
  "v_rcp_f32 v226, v226\n" \
  "v_rcp_f32 v227, v227\n" \
  "v_rcp_f32 v228, v228\n" \
  "v_rcp_f32 v229, v229\n" \
  "v_rcp_f32 v230, v230\n" \
  "v_rcp_f32 v231, v231\n" \
  "s_nop 0\n" \
  "v_pk_mul_f32 v[172:173], v[172:173], v[224:225]\n" \
  "v_pk_mul_f32 v[174:175], v[174:175], v[226:227]\n" \
  "v_pk_mul_f32 v[188:189], v[188:189], v[228:229]\n" \
  "v_pk_mul_f32 v[190:191], v[190:191], v[230:231]\n" \
  "v_cvt_pk_bf16_f32 v140, v172, v173\n" \
  "v_cvt_pk_bf16_f32 v141, v174, v175\n" \
  "v_cvt_pk_bf16_f32 v142, v188, v189\n" \
  "v_cvt_pk_bf16_f32 v143, v190, v191\n" \
  "buffer_store_dwordx4 v[140:143], v246, s[44:47], s57 offen offset:1024 sc1\n" \
  "v_pk_mul_f32 v[224:225], v[192:193], v[192:193]\n" \
  "v_pk_mul_f32 v[226:227], v[194:195], v[194:195]\n" \
  "v_pk_mul_f32 v[228:229], v[200:201], v[200:201]\n" \
  "v_pk_mul_f32 v[230:231], v[202:203], v[202:203]\n" \
  "v_pk_fma_f32 v[224:225], v[224:225], v[234:235], v[232:233]\n" \
  "v_pk_fma_f32 v[226:227], v[226:227], v[234:235], v[232:233]\n" \
  "v_pk_fma_f32 v[228:229], v[228:229], v[234:235], v[232:233]\n" \
  "v_pk_fma_f32 v[230:231], v[230:231], v[234:235], v[232:233]\n" \
  "v_pk_mul_f32 v[224:225], v[224:225], v[192:193]\n" \
  "v_pk_mul_f32 v[226:227], v[226:227], v[194:195]\n" \
  "v_pk_mul_f32 v[228:229], v[228:229], v[200:201]\n" \
  "v_pk_mul_f32 v[230:231], v[230:231], v[202:203]\n" \
  "v_exp_f32 v224, v224\n" \
  "v_exp_f32 v225, v225\n" \
  "v_exp_f32 v226, v226\n" \
  "v_exp_f32 v227, v227\n" \
  "v_exp_f32 v228, v228\n" \
  "v_exp_f32 v229, v229\n" \
  "v_exp_f32 v230, v230\n" \
  "v_exp_f32 v231, v231\n" \
  "v_pk_mul_f32 v[208:209], v[192:193], v[208:209]\n" \
  "v_pk_mul_f32 v[210:211], v[194:195], v[210:211]\n" \
  "v_pk_mul_f32 v[216:217], v[200:201], v[216:217]\n" \
  "v_pk_mul_f32 v[218:219], v[202:203], v[218:219]\n" \
  "v_pk_add_f32 v[224:225], v[224:225], 1.0 op_sel_hi:[1,0]\n" \
  "v_pk_add_f32 v[226:227], v[226:227], 1.0 op_sel_hi:[1,0]\n" \
  "v_pk_add_f32 v[228:229], v[228:229], 1.0 op_sel_hi:[1,0]\n" \
  "v_pk_add_f32 v[230:231], v[230:231], 1.0 op_sel_hi:[1,0]\n" \
  "v_rcp_f32 v224, v224\n" \
  "v_rcp_f32 v225, v225\n" \
  "v_rcp_f32 v226, v226\n" \
  "v_rcp_f32 v227, v227\n" \
  "v_rcp_f32 v228, v228\n" \
  "v_rcp_f32 v229, v229\n" \
  "v_rcp_f32 v230, v230\n" \
  "v_rcp_f32 v231, v231\n" \
  "s_nop 0\n" \
  "v_pk_mul_f32 v[208:209], v[208:209], v[224:225]\n" \
  "v_pk_mul_f32 v[210:211], v[210:211], v[226:227]\n" \
  "v_pk_mul_f32 v[216:217], v[216:217], v[228:229]\n" \
  "v_pk_mul_f32 v[218:219], v[218:219], v[230:231]\n" \
  "v_cvt_pk_bf16_f32 v192, v208, v209\n" \
  "v_cvt_pk_bf16_f32 v193, v210, v211\n" \
  "v_cvt_pk_bf16_f32 v194, v216, v217\n" \
  "v_cvt_pk_bf16_f32 v195, v218, v219\n" \
  "buffer_store_dwordx4 v[192:195], v246, s[44:47], s58 offen offset:0 sc1\n" \
  "v_pk_mul_f32 v[224:225], v[196:197], v[196:197]\n" \
  "v_pk_mul_f32 v[226:227], v[198:199], v[198:199]\n" \
  "v_pk_mul_f32 v[228:229], v[204:205], v[204:205]\n" \
  "v_pk_mul_f32 v[230:231], v[206:207], v[206:207]\n" \
  "v_pk_fma_f32 v[224:225], v[224:225], v[234:235], v[232:233]\n" \
  "v_pk_fma_f32 v[226:227], v[226:227], v[234:235], v[232:233]\n" \
  "v_pk_fma_f32 v[228:229], v[228:229], v[234:235], v[232:233]\n" \
  "v_pk_fma_f32 v[230:231], v[230:231], v[234:235], v[232:233]\n" \
  "v_pk_mul_f32 v[224:225], v[224:225], v[196:197]\n" \
  "v_pk_mul_f32 v[226:227], v[226:227], v[198:199]\n" \
  "v_pk_mul_f32 v[228:229], v[228:229], v[204:205]\n" \
  "v_pk_mul_f32 v[230:231], v[230:231], v[206:207]\n" \
  "v_exp_f32 v224, v224\n" \
  "v_exp_f32 v225, v225\n" \
  "v_exp_f32 v226, v226\n" \
  "v_exp_f32 v227, v227\n" \
  "v_exp_f32 v228, v228\n" \
  "v_exp_f32 v229, v229\n" \
  "v_exp_f32 v230, v230\n" \
  "v_exp_f32 v231, v231\n" \
  "v_pk_mul_f32 v[212:213], v[196:197], v[212:213]\n" \
  "v_pk_mul_f32 v[214:215], v[198:199], v[214:215]\n" \
  "v_pk_mul_f32 v[220:221], v[204:205], v[220:221]\n" \
  "v_pk_mul_f32 v[222:223], v[206:207], v[222:223]\n" \
  "v_pk_add_f32 v[224:225], v[224:225], 1.0 op_sel_hi:[1,0]\n" \
  "v_pk_add_f32 v[226:227], v[226:227], 1.0 op_sel_hi:[1,0]\n" \
  "v_pk_add_f32 v[228:229], v[228:229], 1.0 op_sel_hi:[1,0]\n" \
  "v_pk_add_f32 v[230:231], v[230:231], 1.0 op_sel_hi:[1,0]\n" \
  "v_rcp_f32 v224, v224\n" \
  "v_rcp_f32 v225, v225\n" \
  "v_rcp_f32 v226, v226\n" \
  "v_rcp_f32 v227, v227\n" \
  "v_rcp_f32 v228, v228\n" \
  "v_rcp_f32 v229, v229\n" \
  "v_rcp_f32 v230, v230\n" \
  "v_rcp_f32 v231, v231\n" \
  "s_nop 0\n" \
  "v_pk_mul_f32 v[212:213], v[212:213], v[224:225]\n" \
  "v_pk_mul_f32 v[214:215], v[214:215], v[226:227]\n" \
  "v_pk_mul_f32 v[220:221], v[220:221], v[228:229]\n" \
  "v_pk_mul_f32 v[222:223], v[222:223], v[230:231]\n" \
  "v_cvt_pk_bf16_f32 v196, v212, v213\n" \
  "v_cvt_pk_bf16_f32 v197, v214, v215\n" \
  "v_cvt_pk_bf16_f32 v198, v220, v221\n" \
  "v_cvt_pk_bf16_f32 v199, v222, v223\n" \
  "buffer_store_dwordx4 v[196:199], v246, s[44:47], s58 offen offset:1024 sc1\n" \
  "s_waitcnt vmcnt(0) lgkmcnt(0)\n" \
  ""
#define G2_ASM \
  "s_add_u32 m0, s64, 0\n" \
  "s_nop 0\n" \
  "buffer_load_dwordx4 v80, s[36:39], s65 offen offset:0 lds\n" \
  "buffer_load_dwordx4 a[0:3], v84, s[40:43], s66 offen offset:0 sc1\n" \
  "buffer_load_dwordx4 v81, s[36:39], s65 offen offset:1024 lds\n" \
  "buffer_load_dwordx4 a[4:7], v84, s[40:43], s66 offen offset:1024 sc1\n" \
  "buffer_load_dwordx4 v82, s[36:39], s65 offen offset:2048 lds\n" \
  "buffer_load_dwordx4 a[8:11], v84, s[40:43], s66 offen offset:2048 sc1\n" \
  "buffer_load_dwordx4 v83, s[36:39], s65 offen offset:3072 lds\n" \
  "buffer_load_dwordx4 a[12:15], v84, s[40:43], s66 offen offset:3072 sc1\n" \
  "s_add_u32 s65, s65, 131072\n" \
  "s_add_u32 s66, s66, 4096\n" \
  "v_mov_b32 v0, 0\n" \
  "v_mov_b32 v1, 0\n" \
  "v_mov_b32 v2, 0\n" \
  "v_mov_b32 v3, 0\n" \
  "v_mov_b32 v4, 0\n" \
  "v_mov_b32 v5, 0\n" \
  "v_mov_b32 v6, 0\n" \
  "v_mov_b32 v7, 0\n" \
  "v_mov_b32 v8, 0\n" \
  "v_mov_b32 v9, 0\n" \
  "v_mov_b32 v10, 0\n" \
  "v_mov_b32 v11, 0\n" \
  "v_mov_b32 v12, 0\n" \
  "v_mov_b32 v13, 0\n" \
  "v_mov_b32 v14, 0\n" \
  "v_mov_b32 v15, 0\n" \
  "v_mov_b32 v16, 0\n" \
  "v_mov_b32 v17, 0\n" \
  "v_mov_b32 v18, 0\n" \
  "v_mov_b32 v19, 0\n" \
  "v_mov_b32 v20, 0\n" \
  "v_mov_b32 v21, 0\n" \
  "v_mov_b32 v22, 0\n" \
  "v_mov_b32 v23, 0\n" \
  "v_mov_b32 v24, 0\n" \
  "v_mov_b32 v25, 0\n" \
  "v_mov_b32 v26, 0\n" \
  "v_mov_b32 v27, 0\n" \
  "v_mov_b32 v28, 0\n" \
  "v_mov_b32 v29, 0\n" \
  "v_mov_b32 v30, 0\n" \
  "v_mov_b32 v31, 0\n" \
  "v_mov_b32 v32, 0\n" \
  "v_mov_b32 v33, 0\n" \
  "v_mov_b32 v34, 0\n" \
  "v_mov_b32 v35, 0\n" \
  "v_mov_b32 v36, 0\n" \
  "v_mov_b32 v37, 0\n" \
  "v_mov_b32 v38, 0\n" \
  "v_mov_b32 v39, 0\n" \
  "v_mov_b32 v40, 0\n" \
  "v_mov_b32 v41, 0\n" \
  "v_mov_b32 v42, 0\n" \
  "v_mov_b32 v43, 0\n" \
  "v_mov_b32 v44, 0\n" \
  "v_mov_b32 v45, 0\n" \
  "v_mov_b32 v46, 0\n" \
  "v_mov_b32 v47, 0\n" \
  "v_mov_b32 v48, 0\n" \
  "v_mov_b32 v49, 0\n" \
  "v_mov_b32 v50, 0\n" \
  "v_mov_b32 v51, 0\n" \
  "v_mov_b32 v52, 0\n" \
  "v_mov_b32 v53, 0\n" \
  "v_mov_b32 v54, 0\n" \
  "v_mov_b32 v55, 0\n" \
  "v_mov_b32 v56, 0\n" \
  "v_mov_b32 v57, 0\n" \
  "v_mov_b32 v58, 0\n" \
  "v_mov_b32 v59, 0\n" \
  "v_mov_b32 v60, 0\n" \
  "v_mov_b32 v61, 0\n" \
  "v_mov_b32 v62, 0\n" \
  "v_mov_b32 v63, 0\n" \
  "s_add_u32 m0, s64, 16384\n" \
  "s_nop 0\n" \
  "buffer_load_dwordx4 v80, s[36:39], s65 offen offset:0 lds\n" \
  "buffer_load_dwordx4 a[16:19], v84, s[40:43], s66 offen offset:0 sc1\n" \
  "buffer_load_dwordx4 v81, s[36:39], s65 offen offset:1024 lds\n" \
  "buffer_load_dwordx4 a[20:23], v84, s[40:43], s66 offen offset:1024 sc1\n" \
  "buffer_load_dwordx4 v82, s[36:39], s65 offen offset:2048 lds\n" \
  "buffer_load_dwordx4 a[24:27], v84, s[40:43], s66 offen offset:2048 sc1\n" \
  "buffer_load_dwordx4 v83, s[36:39], s65 offen offset:3072 lds\n" \
  "buffer_load_dwordx4 a[28:31], v84, s[40:43], s66 offen offset:3072 sc1\n" \
  "s_add_u32 s65, s65, 131072\n" \
  "s_add_u32 s66, s66, 4096\n" \
  "s_add_u32 m0, s64, 32768\n" \
  "s_nop 0\n" \
  "buffer_load_dwordx4 v80, s[36:39], s65 offen offset:0 lds\n" \
  "buffer_load_dwordx4 a[32:35], v84, s[40:43], s66 offen offset:0 sc1\n" \
  "buffer_load_dwordx4 v81, s[36:39], s65 offen offset:1024 lds\n" \
  "buffer_load_dwordx4 a[36:39], v84, s[40:43], s66 offen offset:1024 sc1\n" \
  "buffer_load_dwordx4 v82, s[36:39], s65 offen offset:2048 lds\n" \
  "buffer_load_dwordx4 a[40:43], v84, s[40:43], s66 offen offset:2048 sc1\n" \
  "buffer_load_dwordx4 v83, s[36:39], s65 offen offset:3072 lds\n" \
  "buffer_load_dwordx4 a[44:47], v84, s[40:43], s66 offen offset:3072 sc1\n" \
  "s_add_u32 s65, s65, 131072\n" \
  "s_add_u32 s66, s66, 4096\n" \
  "s_mov_b32 s67, 0\n" \
  "s_waitcnt vmcnt(16)\n" \
  "s_barrier\n" \
  "ds_read_b64_tr_b16 a[112:113], v64 offset:0\n" \
  "ds_read_b64_tr_b16 a[114:115], v64 offset:4096\n" \
  "ds_read_b64_tr_b16 a[116:117], v65 offset:0\n" \
  "ds_read_b64_tr_b16 a[118:119], v65 offset:4096\n" \
  "ds_read_b64_tr_b16 a[120:121], v66 offset:0\n" \
  "ds_read_b64_tr_b16 a[122:123], v66 offset:4096\n" \
  "ds_read_b64_tr_b16 a[124:125], v67 offset:0\n" \
  "ds_read_b64_tr_b16 a[126:127], v67 offset:4096\n" \
  "ds_read_b64_tr_b16 a[128:129], v68 offset:0\n" \
  "ds_read_b64_tr_b16 a[130:131], v68 offset:4096\n" \
  "L_G2_loop_%=:\n" \
  "s_waitcnt lgkmcnt(6)\n" \
  "v_mfma_f32_16x16x32_bf16 v[0:3], a[112:115], a[0:3], v[0:3]\n" \
  "s_add_u32 m0, s64, 49152\n" \
  "ds_read_b64_tr_b16 a[132:133], v69 offset:0\n" \
  "v_mfma_f32_16x16x32_bf16 v[4:7], a[112:115], a[4:7], v[4:7]\n" \
  "ds_read_b64_tr_b16 a[134:135], v69 offset:4096\n" \
  "v_mfma_f32_16x16x32_bf16 v[8:11], a[116:119], a[0:3], v[8:11]\n" \
  "ds_read_b64_tr_b16 a[136:137], v70 offset:0\n" \
  "buffer_load_dwordx4 v80, s[36:39], s65 offen offset:0 lds\n" \
  "v_mfma_f32_16x16x32_bf16 v[12:15], a[116:119], a[4:7], v[12:15]\n" \
  "ds_read_b64_tr_b16 a[138:139], v70 offset:4096\n" \
  "s_waitcnt lgkmcnt(6)\n" \
  "v_mfma_f32_16x16x32_bf16 v[16:19], a[120:123], a[0:3], v[16:19]\n" \
  "ds_read_b64_tr_b16 a[140:141], v71 offset:0\n" \
  "buffer_load_dwordx4 a[48:51], v84, s[40:43], s66 offen offset:0 sc1\n" \
  "v_mfma_f32_16x16x32_bf16 v[20:23], a[120:123], a[4:7], v[20:23]\n" \
  "ds_read_b64_tr_b16 a[142:143], v71 offset:4096\n" \
  "v_mfma_f32_16x16x32_bf16 v[24:27], a[124:127], a[0:3], v[24:27]\n" \
  "ds_read_b64_tr_b16 a[144:145], v64 offset:8192\n" \
  "buffer_load_dwordx4 v81, s[36:39], s65 offen offset:1024 lds\n" \
  "v_mfma_f32_16x16x32_bf16 v[28:31], a[124:127], a[4:7], v[28:31]\n" \
  "ds_read_b64_tr_b16 a[146:147], v64 offset:12288\n" \
  "s_waitcnt lgkmcnt(6)\n" \
  "v_mfma_f32_16x16x32_bf16 v[32:35], a[128:131], a[0:3], v[32:35]\n" \
  "ds_read_b64_tr_b16 a[148:149], v65 offset:8192\n" \
  "buffer_load_dwordx4 a[52:55], v84, s[40:43], s66 offen offset:1024 sc1\n" \
  "v_mfma_f32_16x16x32_bf16 v[36:39], a[128:131], a[4:7], v[36:39]\n" \
  "ds_read_b64_tr_b16 a[150:151], v65 offset:12288\n" \
  "v_mfma_f32_16x16x32_bf16 v[40:43], a[132:135], a[0:3], v[40:43]\n" \
  "ds_read_b64_tr_b16 a[152:153], v66 offset:8192\n" \
  "buffer_load_dwordx4 v82, s[36:39], s65 offen offset:2048 lds\n" \
  "v_mfma_f32_16x16x32_bf16 v[44:47], a[132:135], a[4:7], v[44:47]\n" \
  "ds_read_b64_tr_b16 a[154:155], v66 offset:12288\n" \
  "s_waitcnt lgkmcnt(6)\n" \
  "v_mfma_f32_16x16x32_bf16 v[48:51], a[136:139], a[0:3], v[48:51]\n" \
  "ds_read_b64_tr_b16 a[156:157], v67 offset:8192\n" \
  "buffer_load_dwordx4 a[56:59], v84, s[40:43], s66 offen offset:2048 sc1\n" \
  "v_mfma_f32_16x16x32_bf16 v[52:55], a[136:139], a[4:7], v[52:55]\n" \
  "ds_read_b64_tr_b16 a[158:159], v67 offset:12288\n" \
  "v_mfma_f32_16x16x32_bf16 v[56:59], a[140:143], a[0:3], v[56:59]\n" \
  "ds_read_b64_tr_b16 a[160:161], v68 offset:8192\n" \
  "buffer_load_dwordx4 v83, s[36:39], s65 offen offset:3072 lds\n" \
  "v_mfma_f32_16x16x32_bf16 v[60:63], a[140:143], a[4:7], v[60:63]\n" \
  "ds_read_b64_tr_b16 a[162:163], v68 offset:12288\n" \
  "s_waitcnt lgkmcnt(6)\n" \
  "v_mfma_f32_16x16x32_bf16 v[0:3], a[144:147], a[8:11], v[0:3]\n" \
  "ds_read_b64_tr_b16 a[164:165], v69 offset:8192\n" \
  "buffer_load_dwordx4 a[60:63], v84, s[40:43], s66 offen offset:3072 sc1\n" \
  "v_mfma_f32_16x16x32_bf16 v[4:7], a[144:147], a[12:15], v[4:7]\n" \
  "ds_read_b64_tr_b16 a[166:167], v69 offset:12288\n" \
  "s_add_u32 s65, s65, 131072\n" \
  "v_mfma_f32_16x16x32_bf16 v[8:11], a[148:151], a[8:11], v[8:11]\n" \
  "ds_read_b64_tr_b16 a[168:169], v70 offset:8192\n" \
  "s_add_u32 s66, s66, 4096\n" \
  "v_mfma_f32_16x16x32_bf16 v[12:15], a[148:151], a[12:15], v[12:15]\n" \
  "ds_read_b64_tr_b16 a[170:171], v70 offset:12288\n" \
  "s_waitcnt lgkmcnt(6)\n" \
  "v_mfma_f32_16x16x32_bf16 v[16:19], a[152:155], a[8:11], v[16:19]\n" \
  "ds_read_b64_tr_b16 a[172:173], v71 offset:8192\n" \
  "v_mfma_f32_16x16x32_bf16 v[20:23], a[152:155], a[12:15], v[20:23]\n" \
  "ds_read_b64_tr_b16 a[174:175], v71 offset:12288\n" \
  "s_waitcnt vmcnt(16)\n" \
  "s_barrier\n" \
  "v_mfma_f32_16x16x32_bf16 v[24:27], a[156:159], a[8:11], v[24:27]\n" \
  "ds_read_b64_tr_b16 a[112:113], v64 offset:16384\n" \
  "v_mfma_f32_16x16x32_bf16 v[28:31], a[156:159], a[12:15], v[28:31]\n" \
  "ds_read_b64_tr_b16 a[114:115], v64 offset:20480\n" \
  "s_waitcnt lgkmcnt(6)\n" \
  "v_mfma_f32_16x16x32_bf16 v[32:35], a[160:163], a[8:11], v[32:35]\n" \
  "ds_read_b64_tr_b16 a[116:117], v65 offset:16384\n" \
  "v_mfma_f32_16x16x32_bf16 v[36:39], a[160:163], a[12:15], v[36:39]\n" \
  "ds_read_b64_tr_b16 a[118:119], v65 offset:20480\n" \
  "v_mfma_f32_16x16x32_bf16 v[40:43], a[164:167], a[8:11], v[40:43]\n" \
  "ds_read_b64_tr_b16 a[120:121], v66 offset:16384\n" \
  "v_mfma_f32_16x16x32_bf16 v[44:47], a[164:167], a[12:15], v[44:47]\n" \
  "ds_read_b64_tr_b16 a[122:123], v66 offset:20480\n" \
  "s_waitcnt lgkmcnt(6)\n" \
  "v_mfma_f32_16x16x32_bf16 v[48:51], a[168:171], a[8:11], v[48:51]\n" \
  "ds_read_b64_tr_b16 a[124:125], v67 offset:16384\n" \
  "v_mfma_f32_16x16x32_bf16 v[52:55], a[168:171], a[12:15], v[52:55]\n" \
  "ds_read_b64_tr_b16 a[126:127], v67 offset:20480\n" \
  "v_mfma_f32_16x16x32_bf16 v[56:59], a[172:175], a[8:11], v[56:59]\n" \
  "ds_read_b64_tr_b16 a[128:129], v68 offset:16384\n" \
  "v_mfma_f32_16x16x32_bf16 v[60:63], a[172:175], a[12:15], v[60:63]\n" \
  "ds_read_b64_tr_b16 a[130:131], v68 offset:20480\n" \
  "s_waitcnt lgkmcnt(6)\n" \
  "v_mfma_f32_16x16x32_bf16 v[0:3], a[112:115], a[16:19], v[0:3]\n" \
  "s_add_u32 m0, s64, 65536\n" \
  "ds_read_b64_tr_b16 a[132:133], v69 offset:16384\n" \
  "v_mfma_f32_16x16x32_bf16 v[4:7], a[112:115], a[20:23], v[4:7]\n" \
  "ds_read_b64_tr_b16 a[134:135], v69 offset:20480\n" \
  "v_mfma_f32_16x16x32_bf16 v[8:11], a[116:119], a[16:19], v[8:11]\n" \
  "ds_read_b64_tr_b16 a[136:137], v70 offset:16384\n" \
  "buffer_load_dwordx4 v80, s[36:39], s65 offen offset:0 lds\n" \
  "v_mfma_f32_16x16x32_bf16 v[12:15], a[116:119], a[20:23], v[12:15]\n" \
  "ds_read_b64_tr_b16 a[138:139], v70 offset:20480\n" \
  "s_waitcnt lgkmcnt(6)\n" \
  "v_mfma_f32_16x16x32_bf16 v[16:19], a[120:123], a[16:19], v[16:19]\n" \
  "ds_read_b64_tr_b16 a[140:141], v71 offset:16384\n" \
  "buffer_load_dwordx4 a[64:67], v84, s[40:43], s66 offen offset:0 sc1\n" \
  "v_mfma_f32_16x16x32_bf16 v[20:23], a[120:123], a[20:23], v[20:23]\n" \
  "ds_read_b64_tr_b16 a[142:143], v71 offset:20480\n" \
  "v_mfma_f32_16x16x32_bf16 v[24:27], a[124:127], a[16:19], v[24:27]\n" \
  "ds_read_b64_tr_b16 a[144:145], v64 offset:24576\n" \
  "buffer_load_dwordx4 v81, s[36:39], s65 offen offset:1024 lds\n" \
  "v_mfma_f32_16x16x32_bf16 v[28:31], a[124:127], a[20:23], v[28:31]\n" \
  "ds_read_b64_tr_b16 a[146:147], v64 offset:28672\n" \
  "s_waitcnt lgkmcnt(6)\n" \
  "v_mfma_f32_16x16x32_bf16 v[32:35], a[128:131], a[16:19], v[32:35]\n" \
  "ds_read_b64_tr_b16 a[148:149], v65 offset:24576\n" \
  "buffer_load_dwordx4 a[68:71], v84, s[40:43], s66 offen offset:1024 sc1\n" \
  "v_mfma_f32_16x16x32_bf16 v[36:39], a[128:131], a[20:23], v[36:39]\n" \
  "ds_read_b64_tr_b16 a[150:151], v65 offset:28672\n" \
  "v_mfma_f32_16x16x32_bf16 v[40:43], a[132:135], a[16:19], v[40:43]\n" \
  "ds_read_b64_tr_b16 a[152:153], v66 offset:24576\n" \
  "buffer_load_dwordx4 v82, s[36:39], s65 offen offset:2048 lds\n" \
  "v_mfma_f32_16x16x32_bf16 v[44:47], a[132:135], a[20:23], v[44:47]\n" \
  "ds_read_b64_tr_b16 a[154:155], v66 offset:28672\n" \
  "s_waitcnt lgkmcnt(6)\n" \
  "v_mfma_f32_16x16x32_bf16 v[48:51], a[136:139], a[16:19], v[48:51]\n" \
  "ds_read_b64_tr_b16 a[156:157], v67 offset:24576\n" \
  "buffer_load_dwordx4 a[72:75], v84, s[40:43], s66 offen offset:2048 sc1\n" \
  "v_mfma_f32_16x16x32_bf16 v[52:55], a[136:139], a[20:23], v[52:55]\n" \
  "ds_read_b64_tr_b16 a[158:159], v67 offset:28672\n" \
  "v_mfma_f32_16x16x32_bf16 v[56:59], a[140:143], a[16:19], v[56:59]\n" \
  "ds_read_b64_tr_b16 a[160:161], v68 offset:24576\n" \
  "buffer_load_dwordx4 v83, s[36:39], s65 offen offset:3072 lds\n" \
  "v_mfma_f32_16x16x32_bf16 v[60:63], a[140:143], a[20:23], v[60:63]\n" \
  "ds_read_b64_tr_b16 a[162:163], v68 offset:28672\n" \
  "s_waitcnt lgkmcnt(6)\n" \
  "v_mfma_f32_16x16x32_bf16 v[0:3], a[144:147], a[24:27], v[0:3]\n" \
  "ds_read_b64_tr_b16 a[164:165], v69 offset:24576\n" \
  "buffer_load_dwordx4 a[76:79], v84, s[40:43], s66 offen offset:3072 sc1\n" \
  "v_mfma_f32_16x16x32_bf16 v[4:7], a[144:147], a[28:31], v[4:7]\n" \
  "ds_read_b64_tr_b16 a[166:167], v69 offset:28672\n" \
  "s_add_u32 s65, s65, 131072\n" \
  "v_mfma_f32_16x16x32_bf16 v[8:11], a[148:151], a[24:27], v[8:11]\n" \
  "ds_read_b64_tr_b16 a[168:169], v70 offset:24576\n" \
  "s_add_u32 s66, s66, 4096\n" \
  "v_mfma_f32_16x16x32_bf16 v[12:15], a[148:151], a[28:31], v[12:15]\n" \
  "ds_read_b64_tr_b16 a[170:171], v70 offset:28672\n" \
  "s_waitcnt lgkmcnt(6)\n" \
  "v_mfma_f32_16x16x32_bf16 v[16:19], a[152:155], a[24:27], v[16:19]\n" \
  "ds_read_b64_tr_b16 a[172:173], v71 offset:24576\n" \
  "v_mfma_f32_16x16x32_bf16 v[20:23], a[152:155], a[28:31], v[20:23]\n" \
  "ds_read_b64_tr_b16 a[174:175], v71 offset:28672\n" \
  "s_waitcnt vmcnt(16)\n" \
  "s_barrier\n" \
  "v_mfma_f32_16x16x32_bf16 v[24:27], a[156:159], a[24:27], v[24:27]\n" \
  "ds_read_b64_tr_b16 a[112:113], v64 offset:32768\n" \
  "v_mfma_f32_16x16x32_bf16 v[28:31], a[156:159], a[28:31], v[28:31]\n" \
  "ds_read_b64_tr_b16 a[114:115], v64 offset:36864\n" \
  "s_waitcnt lgkmcnt(6)\n" \
  "v_mfma_f32_16x16x32_bf16 v[32:35], a[160:163], a[24:27], v[32:35]\n" \
  "ds_read_b64_tr_b16 a[116:117], v65 offset:32768\n" \
  "v_mfma_f32_16x16x32_bf16 v[36:39], a[160:163], a[28:31], v[36:39]\n" \
  "ds_read_b64_tr_b16 a[118:119], v65 offset:36864\n" \
  "v_mfma_f32_16x16x32_bf16 v[40:43], a[164:167], a[24:27], v[40:43]\n" \
  "ds_read_b64_tr_b16 a[120:121], v66 offset:32768\n" \
  "v_mfma_f32_16x16x32_bf16 v[44:47], a[164:167], a[28:31], v[44:47]\n" \
  "ds_read_b64_tr_b16 a[122:123], v66 offset:36864\n" \
  "s_waitcnt lgkmcnt(6)\n" \
  "v_mfma_f32_16x16x32_bf16 v[48:51], a[168:171], a[24:27], v[48:51]\n" \
  "ds_read_b64_tr_b16 a[124:125], v67 offset:32768\n" \
  "v_mfma_f32_16x16x32_bf16 v[52:55], a[168:171], a[28:31], v[52:55]\n" \
  "ds_read_b64_tr_b16 a[126:127], v67 offset:36864\n" \
  "v_mfma_f32_16x16x32_bf16 v[56:59], a[172:175], a[24:27], v[56:59]\n" \
  "ds_read_b64_tr_b16 a[128:129], v68 offset:32768\n" \
  "v_mfma_f32_16x16x32_bf16 v[60:63], a[172:175], a[28:31], v[60:63]\n" \
  "ds_read_b64_tr_b16 a[130:131], v68 offset:36864\n" \
  "s_waitcnt lgkmcnt(6)\n" \
  "v_mfma_f32_16x16x32_bf16 v[0:3], a[112:115], a[32:35], v[0:3]\n" \
  "s_add_u32 m0, s64, 81920\n" \
  "ds_read_b64_tr_b16 a[132:133], v69 offset:32768\n" \
  "v_mfma_f32_16x16x32_bf16 v[4:7], a[112:115], a[36:39], v[4:7]\n" \
  "ds_read_b64_tr_b16 a[134:135], v69 offset:36864\n" \
  "v_mfma_f32_16x16x32_bf16 v[8:11], a[116:119], a[32:35], v[8:11]\n" \
  "ds_read_b64_tr_b16 a[136:137], v70 offset:32768\n" \
  "buffer_load_dwordx4 v80, s[36:39], s65 offen offset:0 lds\n" \
  "v_mfma_f32_16x16x32_bf16 v[12:15], a[116:119], a[36:39], v[12:15]\n" \
  "ds_read_b64_tr_b16 a[138:139], v70 offset:36864\n" \
  "s_waitcnt lgkmcnt(6)\n" \
  "v_mfma_f32_16x16x32_bf16 v[16:19], a[120:123], a[32:35], v[16:19]\n" \
  "ds_read_b64_tr_b16 a[140:141], v71 offset:32768\n" \
  "buffer_load_dwordx4 a[80:83], v84, s[40:43], s66 offen offset:0 sc1\n" \
  "v_mfma_f32_16x16x32_bf16 v[20:23], a[120:123], a[36:39], v[20:23]\n" \
  "ds_read_b64_tr_b16 a[142:143], v71 offset:36864\n" \
  "v_mfma_f32_16x16x32_bf16 v[24:27], a[124:127], a[32:35], v[24:27]\n" \
  "ds_read_b64_tr_b16 a[144:145], v64 offset:40960\n" \
  "buffer_load_dwordx4 v81, s[36:39], s65 offen offset:1024 lds\n" \
  "v_mfma_f32_16x16x32_bf16 v[28:31], a[124:127], a[36:39], v[28:31]\n" \
  "ds_read_b64_tr_b16 a[146:147], v64 offset:45056\n" \
  "s_waitcnt lgkmcnt(6)\n" \
  "v_mfma_f32_16x16x32_bf16 v[32:35], a[128:131], a[32:35], v[32:35]\n" \
  "ds_read_b64_tr_b16 a[148:149], v65 offset:40960\n" \
  "buffer_load_dwordx4 a[84:87], v84, s[40:43], s66 offen offset:1024 sc1\n" \
  "v_mfma_f32_16x16x32_bf16 v[36:39], a[128:131], a[36:39], v[36:39]\n" \
  "ds_read_b64_tr_b16 a[150:151], v65 offset:45056\n" \
  "v_mfma_f32_16x16x32_bf16 v[40:43], a[132:135], a[32:35], v[40:43]\n" \
  "ds_read_b64_tr_b16 a[152:153], v66 offset:40960\n" \
  "buffer_load_dwordx4 v82, s[36:39], s65 offen offset:2048 lds\n" \
  "v_mfma_f32_16x16x32_bf16 v[44:47], a[132:135], a[36:39], v[44:47]\n" \
  "ds_read_b64_tr_b16 a[154:155], v66 offset:45056\n" \
  "s_waitcnt lgkmcnt(6)\n" \
  "v_mfma_f32_16x16x32_bf16 v[48:51], a[136:139], a[32:35], v[48:51]\n" \
  "ds_read_b64_tr_b16 a[156:157], v67 offset:40960\n" \
  "buffer_load_dwordx4 a[88:91], v84, s[40:43], s66 offen offset:2048 sc1\n" \
  "v_mfma_f32_16x16x32_bf16 v[52:55], a[136:139], a[36:39], v[52:55]\n" \
  "ds_read_b64_tr_b16 a[158:159], v67 offset:45056\n" \
  "v_mfma_f32_16x16x32_bf16 v[56:59], a[140:143], a[32:35], v[56:59]\n" \
  "ds_read_b64_tr_b16 a[160:161], v68 offset:40960\n" \
  "buffer_load_dwordx4 v83, s[36:39], s65 offen offset:3072 lds\n" \
  "v_mfma_f32_16x16x32_bf16 v[60:63], a[140:143], a[36:39], v[60:63]\n" \
  "ds_read_b64_tr_b16 a[162:163], v68 offset:45056\n" \
  "s_waitcnt lgkmcnt(6)\n" \
  "v_mfma_f32_16x16x32_bf16 v[0:3], a[144:147], a[40:43], v[0:3]\n" \
  "ds_read_b64_tr_b16 a[164:165], v69 offset:40960\n" \
  "buffer_load_dwordx4 a[92:95], v84, s[40:43], s66 offen offset:3072 sc1\n" \
  "v_mfma_f32_16x16x32_bf16 v[4:7], a[144:147], a[44:47], v[4:7]\n" \
  "ds_read_b64_tr_b16 a[166:167], v69 offset:45056\n" \
  "s_add_u32 s65, s65, 131072\n" \
  "v_mfma_f32_16x16x32_bf16 v[8:11], a[148:151], a[40:43], v[8:11]\n" \
  "ds_read_b64_tr_b16 a[168:169], v70 offset:40960\n" \
  "s_add_u32 s66, s66, 4096\n" \
  "v_mfma_f32_16x16x32_bf16 v[12:15], a[148:151], a[44:47], v[12:15]\n" \
  "ds_read_b64_tr_b16 a[170:171], v70 offset:45056\n" \
  "s_waitcnt lgkmcnt(6)\n" \
  "v_mfma_f32_16x16x32_bf16 v[16:19], a[152:155], a[40:43], v[16:19]\n" \
  "ds_read_b64_tr_b16 a[172:173], v71 offset:40960\n" \
  "v_mfma_f32_16x16x32_bf16 v[20:23], a[152:155], a[44:47], v[20:23]\n" \
  "ds_read_b64_tr_b16 a[174:175], v71 offset:45056\n" \
  "s_waitcnt vmcnt(16)\n" \
  "s_barrier\n" \
  "v_mfma_f32_16x16x32_bf16 v[24:27], a[156:159], a[40:43], v[24:27]\n" \
  "ds_read_b64_tr_b16 a[112:113], v64 offset:49152\n" \
  "v_mfma_f32_16x16x32_bf16 v[28:31], a[156:159], a[44:47], v[28:31]\n" \
  "ds_read_b64_tr_b16 a[114:115], v64 offset:53248\n" \
  "s_waitcnt lgkmcnt(6)\n" \
  "v_mfma_f32_16x16x32_bf16 v[32:35], a[160:163], a[40:43], v[32:35]\n" \
  "ds_read_b64_tr_b16 a[116:117], v65 offset:49152\n" \
  "v_mfma_f32_16x16x32_bf16 v[36:39], a[160:163], a[44:47], v[36:39]\n" \
  "ds_read_b64_tr_b16 a[118:119], v65 offset:53248\n" \
  "v_mfma_f32_16x16x32_bf16 v[40:43], a[164:167], a[40:43], v[40:43]\n" \
  "ds_read_b64_tr_b16 a[120:121], v66 offset:49152\n" \
  "v_mfma_f32_16x16x32_bf16 v[44:47], a[164:167], a[44:47], v[44:47]\n" \
  "ds_read_b64_tr_b16 a[122:123], v66 offset:53248\n" \
  "s_waitcnt lgkmcnt(6)\n" \
  "v_mfma_f32_16x16x32_bf16 v[48:51], a[168:171], a[40:43], v[48:51]\n" \
  "ds_read_b64_tr_b16 a[124:125], v67 offset:49152\n" \
  "v_mfma_f32_16x16x32_bf16 v[52:55], a[168:171], a[44:47], v[52:55]\n" \
  "ds_read_b64_tr_b16 a[126:127], v67 offset:53248\n" \
  "v_mfma_f32_16x16x32_bf16 v[56:59], a[172:175], a[40:43], v[56:59]\n" \
  "ds_read_b64_tr_b16 a[128:129], v68 offset:49152\n" \
  "v_mfma_f32_16x16x32_bf16 v[60:63], a[172:175], a[44:47], v[60:63]\n" \
  "ds_read_b64_tr_b16 a[130:131], v68 offset:53248\n" \
  "s_waitcnt lgkmcnt(6)\n" \
  "v_mfma_f32_16x16x32_bf16 v[0:3], a[112:115], a[48:51], v[0:3]\n" \
  "s_add_u32 m0, s64, 98304\n" \
  "ds_read_b64_tr_b16 a[132:133], v69 offset:49152\n" \
  "v_mfma_f32_16x16x32_bf16 v[4:7], a[112:115], a[52:55], v[4:7]\n" \
  "ds_read_b64_tr_b16 a[134:135], v69 offset:53248\n" \
  "v_mfma_f32_16x16x32_bf16 v[8:11], a[116:119], a[48:51], v[8:11]\n" \
  "ds_read_b64_tr_b16 a[136:137], v70 offset:49152\n" \
  "buffer_load_dwordx4 v80, s[36:39], s65 offen offset:0 lds\n" \
  "v_mfma_f32_16x16x32_bf16 v[12:15], a[116:119], a[52:55], v[12:15]\n" \
  "ds_read_b64_tr_b16 a[138:139], v70 offset:53248\n" \
  "s_waitcnt lgkmcnt(6)\n" \
  "v_mfma_f32_16x16x32_bf16 v[16:19], a[120:123], a[48:51], v[16:19]\n" \
  "ds_read_b64_tr_b16 a[140:141], v71 offset:49152\n" \
  "buffer_load_dwordx4 a[96:99], v84, s[40:43], s66 offen offset:0 sc1\n" \
  "v_mfma_f32_16x16x32_bf16 v[20:23], a[120:123], a[52:55], v[20:23]\n" \
  "ds_read_b64_tr_b16 a[142:143], v71 offset:53248\n" \
  "v_mfma_f32_16x16x32_bf16 v[24:27], a[124:127], a[48:51], v[24:27]\n" \
  "ds_read_b64_tr_b16 a[144:145], v64 offset:57344\n" \
  "buffer_load_dwordx4 v81, s[36:39], s65 offen offset:1024 lds\n" \
  "v_mfma_f32_16x16x32_bf16 v[28:31], a[124:127], a[52:55], v[28:31]\n" \
  "ds_read_b64_tr_b16 a[146:147], v64 offset:61440\n" \
  "s_waitcnt lgkmcnt(6)\n" \
  "v_mfma_f32_16x16x32_bf16 v[32:35], a[128:131], a[48:51], v[32:35]\n" \
  "ds_read_b64_tr_b16 a[148:149], v65 offset:57344\n" \
  "buffer_load_dwordx4 a[100:103], v84, s[40:43], s66 offen offset:1024 sc1\n" \
  "v_mfma_f32_16x16x32_bf16 v[36:39], a[128:131], a[52:55], v[36:39]\n" \
  "ds_read_b64_tr_b16 a[150:151], v65 offset:61440\n" \
  "v_mfma_f32_16x16x32_bf16 v[40:43], a[132:135], a[48:51], v[40:43]\n" \
  "ds_read_b64_tr_b16 a[152:153], v66 offset:57344\n" \
  "buffer_load_dwordx4 v82, s[36:39], s65 offen offset:2048 lds\n" \
  "v_mfma_f32_16x16x32_bf16 v[44:47], a[132:135], a[52:55], v[44:47]\n" \
  "ds_read_b64_tr_b16 a[154:155], v66 offset:61440\n" \
  "s_waitcnt lgkmcnt(6)\n" \
  "v_mfma_f32_16x16x32_bf16 v[48:51], a[136:139], a[48:51], v[48:51]\n" \
  "ds_read_b64_tr_b16 a[156:157], v67 offset:57344\n" \
  "buffer_load_dwordx4 a[104:107], v84, s[40:43], s66 offen offset:2048 sc1\n" \
  "v_mfma_f32_16x16x32_bf16 v[52:55], a[136:139], a[52:55], v[52:55]\n" \
  "ds_read_b64_tr_b16 a[158:159], v67 offset:61440\n" \
  "v_mfma_f32_16x16x32_bf16 v[56:59], a[140:143], a[48:51], v[56:59]\n" \
  "ds_read_b64_tr_b16 a[160:161], v68 offset:57344\n" \
  "buffer_load_dwordx4 v83, s[36:39], s65 offen offset:3072 lds\n" \
  "v_mfma_f32_16x16x32_bf16 v[60:63], a[140:143], a[52:55], v[60:63]\n" \
  "ds_read_b64_tr_b16 a[162:163], v68 offset:61440\n" \
  "s_waitcnt lgkmcnt(6)\n" \
  "v_mfma_f32_16x16x32_bf16 v[0:3], a[144:147], a[56:59], v[0:3]\n" \
  "ds_read_b64_tr_b16 a[164:165], v69 offset:57344\n" \
  "buffer_load_dwordx4 a[108:111], v84, s[40:43], s66 offen offset:3072 sc1\n" \
  "v_mfma_f32_16x16x32_bf16 v[4:7], a[144:147], a[60:63], v[4:7]\n" \
  "ds_read_b64_tr_b16 a[166:167], v69 offset:61440\n" \
  "s_add_u32 s65, s65, 131072\n" \
  "v_mfma_f32_16x16x32_bf16 v[8:11], a[148:151], a[56:59], v[8:11]\n" \
  "ds_read_b64_tr_b16 a[168:169], v70 offset:57344\n" \
  "s_add_u32 s66, s66, 4096\n" \
  "v_mfma_f32_16x16x32_bf16 v[12:15], a[148:151], a[60:63], v[12:15]\n" \
  "ds_read_b64_tr_b16 a[170:171], v70 offset:61440\n" \
  "s_waitcnt lgkmcnt(6)\n" \
  "v_mfma_f32_16x16x32_bf16 v[16:19], a[152:155], a[56:59], v[16:19]\n" \
  "ds_read_b64_tr_b16 a[172:173], v71 offset:57344\n" \
  "v_mfma_f32_16x16x32_bf16 v[20:23], a[152:155], a[60:63], v[20:23]\n" \
  "ds_read_b64_tr_b16 a[174:175], v71 offset:61440\n" \
  "s_waitcnt vmcnt(16)\n" \
  "s_barrier\n" \
  "v_mfma_f32_16x16x32_bf16 v[24:27], a[156:159], a[56:59], v[24:27]\n" \
  "ds_read_b64_tr_b16 a[112:113], v72 offset:0\n" \
  "v_mfma_f32_16x16x32_bf16 v[28:31], a[156:159], a[60:63], v[28:31]\n" \
  "ds_read_b64_tr_b16 a[114:115], v72 offset:4096\n" \
  "s_waitcnt lgkmcnt(6)\n" \
  "v_mfma_f32_16x16x32_bf16 v[32:35], a[160:163], a[56:59], v[32:35]\n" \
  "ds_read_b64_tr_b16 a[116:117], v73 offset:0\n" \
  "v_mfma_f32_16x16x32_bf16 v[36:39], a[160:163], a[60:63], v[36:39]\n" \
  "ds_read_b64_tr_b16 a[118:119], v73 offset:4096\n" \
  "v_mfma_f32_16x16x32_bf16 v[40:43], a[164:167], a[56:59], v[40:43]\n" \
  "ds_read_b64_tr_b16 a[120:121], v74 offset:0\n" \
  "v_mfma_f32_16x16x32_bf16 v[44:47], a[164:167], a[60:63], v[44:47]\n" \
  "ds_read_b64_tr_b16 a[122:123], v74 offset:4096\n" \
  "s_waitcnt lgkmcnt(6)\n" \
  "v_mfma_f32_16x16x32_bf16 v[48:51], a[168:171], a[56:59], v[48:51]\n" \
  "ds_read_b64_tr_b16 a[124:125], v75 offset:0\n" \
  "v_mfma_f32_16x16x32_bf16 v[52:55], a[168:171], a[60:63], v[52:55]\n" \
  "ds_read_b64_tr_b16 a[126:127], v75 offset:4096\n" \
  "v_mfma_f32_16x16x32_bf16 v[56:59], a[172:175], a[56:59], v[56:59]\n" \
  "ds_read_b64_tr_b16 a[128:129], v76 offset:0\n" \
  "v_mfma_f32_16x16x32_bf16 v[60:63], a[172:175], a[60:63], v[60:63]\n" \
  "ds_read_b64_tr_b16 a[130:131], v76 offset:4096\n" \
  "s_waitcnt lgkmcnt(6)\n" \
  "v_mfma_f32_16x16x32_bf16 v[0:3], a[112:115], a[64:67], v[0:3]\n" \
  "s_add_u32 m0, s64, 0\n" \
  "s_cmp_eq_u32 s67, 3\n" \
  "ds_read_b64_tr_b16 a[132:133], v77 offset:0\n" \
  "v_mfma_f32_16x16x32_bf16 v[4:7], a[112:115], a[68:71], v[4:7]\n" \
  "s_cselect_b32 s38, 0, s38\n" \
  "s_cselect_b32 s42, 0, s42\n" \
  "ds_read_b64_tr_b16 a[134:135], v77 offset:4096\n" \
  "v_mfma_f32_16x16x32_bf16 v[8:11], a[116:119], a[64:67], v[8:11]\n" \
  "ds_read_b64_tr_b16 a[136:137], v78 offset:0\n" \
  "buffer_load_dwordx4 v80, s[36:39], s65 offen offset:0 lds\n" \
  "v_mfma_f32_16x16x32_bf16 v[12:15], a[116:119], a[68:71], v[12:15]\n" \
  "ds_read_b64_tr_b16 a[138:139], v78 offset:4096\n" \
  "s_waitcnt lgkmcnt(6)\n" \
  "v_mfma_f32_16x16x32_bf16 v[16:19], a[120:123], a[64:67], v[16:19]\n" \
  "ds_read_b64_tr_b16 a[140:141], v79 offset:0\n" \
  "buffer_load_dwordx4 a[0:3], v84, s[40:43], s66 offen offset:0 sc1\n" \
  "v_mfma_f32_16x16x32_bf16 v[20:23], a[120:123], a[68:71], v[20:23]\n" \
  "ds_read_b64_tr_b16 a[142:143], v79 offset:4096\n" \
  "v_mfma_f32_16x16x32_bf16 v[24:27], a[124:127], a[64:67], v[24:27]\n" \
  "ds_read_b64_tr_b16 a[144:145], v72 offset:8192\n" \
  "buffer_load_dwordx4 v81, s[36:39], s65 offen offset:1024 lds\n" \
  "v_mfma_f32_16x16x32_bf16 v[28:31], a[124:127], a[68:71], v[28:31]\n" \
  "ds_read_b64_tr_b16 a[146:147], v72 offset:12288\n" \
  "s_waitcnt lgkmcnt(6)\n" \
  "v_mfma_f32_16x16x32_bf16 v[32:35], a[128:131], a[64:67], v[32:35]\n" \
  "ds_read_b64_tr_b16 a[148:149], v73 offset:8192\n" \
  "buffer_load_dwordx4 a[4:7], v84, s[40:43], s66 offen offset:1024 sc1\n" \
  "v_mfma_f32_16x16x32_bf16 v[36:39], a[128:131], a[68:71], v[36:39]\n" \
  "ds_read_b64_tr_b16 a[150:151], v73 offset:12288\n" \
  "v_mfma_f32_16x16x32_bf16 v[40:43], a[132:135], a[64:67], v[40:43]\n" \
  "ds_read_b64_tr_b16 a[152:153], v74 offset:8192\n" \
  "buffer_load_dwordx4 v82, s[36:39], s65 offen offset:2048 lds\n" \
  "v_mfma_f32_16x16x32_bf16 v[44:47], a[132:135], a[68:71], v[44:47]\n" \
  "ds_read_b64_tr_b16 a[154:155], v74 offset:12288\n" \
  "s_waitcnt lgkmcnt(6)\n" \
  "v_mfma_f32_16x16x32_bf16 v[48:51], a[136:139], a[64:67], v[48:51]\n" \
  "ds_read_b64_tr_b16 a[156:157], v75 offset:8192\n" \
  "buffer_load_dwordx4 a[8:11], v84, s[40:43], s66 offen offset:2048 sc1\n" \
  "v_mfma_f32_16x16x32_bf16 v[52:55], a[136:139], a[68:71], v[52:55]\n" \
  "ds_read_b64_tr_b16 a[158:159], v75 offset:12288\n" \
  "v_mfma_f32_16x16x32_bf16 v[56:59], a[140:143], a[64:67], v[56:59]\n" \
  "ds_read_b64_tr_b16 a[160:161], v76 offset:8192\n" \
  "buffer_load_dwordx4 v83, s[36:39], s65 offen offset:3072 lds\n" \
  "v_mfma_f32_16x16x32_bf16 v[60:63], a[140:143], a[68:71], v[60:63]\n" \
  "ds_read_b64_tr_b16 a[162:163], v76 offset:12288\n" \
  "s_waitcnt lgkmcnt(6)\n" \
  "v_mfma_f32_16x16x32_bf16 v[0:3], a[144:147], a[72:75], v[0:3]\n" \
  "ds_read_b64_tr_b16 a[164:165], v77 offset:8192\n" \
  "buffer_load_dwordx4 a[12:15], v84, s[40:43], s66 offen offset:3072 sc1\n" \
  "v_mfma_f32_16x16x32_bf16 v[4:7], a[144:147], a[76:79], v[4:7]\n" \
  "ds_read_b64_tr_b16 a[166:167], v77 offset:12288\n" \
  "s_add_u32 s65, s65, 131072\n" \
  "v_mfma_f32_16x16x32_bf16 v[8:11], a[148:151], a[72:75], v[8:11]\n" \
  "ds_read_b64_tr_b16 a[168:169], v78 offset:8192\n" \
  "s_add_u32 s66, s66, 4096\n" \
  "v_mfma_f32_16x16x32_bf16 v[12:15], a[148:151], a[76:79], v[12:15]\n" \
  "ds_read_b64_tr_b16 a[170:171], v78 offset:12288\n" \
  "s_waitcnt lgkmcnt(6)\n" \
  "v_mfma_f32_16x16x32_bf16 v[16:19], a[152:155], a[72:75], v[16:19]\n" \
  "ds_read_b64_tr_b16 a[172:173], v79 offset:8192\n" \
  "v_mfma_f32_16x16x32_bf16 v[20:23], a[152:155], a[76:79], v[20:23]\n" \
  "ds_read_b64_tr_b16 a[174:175], v79 offset:12288\n" \
  "s_waitcnt vmcnt(16)\n" \
  "s_barrier\n" \
  "v_mfma_f32_16x16x32_bf16 v[24:27], a[156:159], a[72:75], v[24:27]\n" \
  "ds_read_b64_tr_b16 a[112:113], v72 offset:16384\n" \
  "v_mfma_f32_16x16x32_bf16 v[28:31], a[156:159], a[76:79], v[28:31]\n" \
  "ds_read_b64_tr_b16 a[114:115], v72 offset:20480\n" \
  "s_waitcnt lgkmcnt(6)\n" \
  "v_mfma_f32_16x16x32_bf16 v[32:35], a[160:163], a[72:75], v[32:35]\n" \
  "ds_read_b64_tr_b16 a[116:117], v73 offset:16384\n" \
  "v_mfma_f32_16x16x32_bf16 v[36:39], a[160:163], a[76:79], v[36:39]\n" \
  "ds_read_b64_tr_b16 a[118:119], v73 offset:20480\n" \
  "v_mfma_f32_16x16x32_bf16 v[40:43], a[164:167], a[72:75], v[40:43]\n" \
  "ds_read_b64_tr_b16 a[120:121], v74 offset:16384\n" \
  "v_mfma_f32_16x16x32_bf16 v[44:47], a[164:167], a[76:79], v[44:47]\n" \
  "ds_read_b64_tr_b16 a[122:123], v74 offset:20480\n" \
  "s_waitcnt lgkmcnt(6)\n" \
  "v_mfma_f32_16x16x32_bf16 v[48:51], a[168:171], a[72:75], v[48:51]\n" \
  "ds_read_b64_tr_b16 a[124:125], v75 offset:16384\n" \
  "v_mfma_f32_16x16x32_bf16 v[52:55], a[168:171], a[76:79], v[52:55]\n" \
  "ds_read_b64_tr_b16 a[126:127], v75 offset:20480\n" \
  "v_mfma_f32_16x16x32_bf16 v[56:59], a[172:175], a[72:75], v[56:59]\n" \
  "ds_read_b64_tr_b16 a[128:129], v76 offset:16384\n" \
  "v_mfma_f32_16x16x32_bf16 v[60:63], a[172:175], a[76:79], v[60:63]\n" \
  "ds_read_b64_tr_b16 a[130:131], v76 offset:20480\n" \
  "s_waitcnt lgkmcnt(6)\n" \
  "v_mfma_f32_16x16x32_bf16 v[0:3], a[112:115], a[80:83], v[0:3]\n" \
  "s_add_u32 m0, s64, 16384\n" \
  "ds_read_b64_tr_b16 a[132:133], v77 offset:16384\n" \
  "v_mfma_f32_16x16x32_bf16 v[4:7], a[112:115], a[84:87], v[4:7]\n" \
  "ds_read_b64_tr_b16 a[134:135], v77 offset:20480\n" \
  "v_mfma_f32_16x16x32_bf16 v[8:11], a[116:119], a[80:83], v[8:11]\n" \
  "ds_read_b64_tr_b16 a[136:137], v78 offset:16384\n" \
  "buffer_load_dwordx4 v80, s[36:39], s65 offen offset:0 lds\n" \
  "v_mfma_f32_16x16x32_bf16 v[12:15], a[116:119], a[84:87], v[12:15]\n" \
  "ds_read_b64_tr_b16 a[138:139], v78 offset:20480\n" \
  "s_waitcnt lgkmcnt(6)\n" \
  "v_mfma_f32_16x16x32_bf16 v[16:19], a[120:123], a[80:83], v[16:19]\n" \
  "ds_read_b64_tr_b16 a[140:141], v79 offset:16384\n" \
  "buffer_load_dwordx4 a[16:19], v84, s[40:43], s66 offen offset:0 sc1\n" \
  "v_mfma_f32_16x16x32_bf16 v[20:23], a[120:123], a[84:87], v[20:23]\n" \
  "ds_read_b64_tr_b16 a[142:143], v79 offset:20480\n" \
  "v_mfma_f32_16x16x32_bf16 v[24:27], a[124:127], a[80:83], v[24:27]\n" \
  "ds_read_b64_tr_b16 a[144:145], v72 offset:24576\n" \
  "buffer_load_dwordx4 v81, s[36:39], s65 offen offset:1024 lds\n" \
  "v_mfma_f32_16x16x32_bf16 v[28:31], a[124:127], a[84:87], v[28:31]\n" \
  "ds_read_b64_tr_b16 a[146:147], v72 offset:28672\n" \
  "s_waitcnt lgkmcnt(6)\n" \
  "v_mfma_f32_16x16x32_bf16 v[32:35], a[128:131], a[80:83], v[32:35]\n" \
  "ds_read_b64_tr_b16 a[148:149], v73 offset:24576\n" \
  "buffer_load_dwordx4 a[20:23], v84, s[40:43], s66 offen offset:1024 sc1\n" \
  "v_mfma_f32_16x16x32_bf16 v[36:39], a[128:131], a[84:87], v[36:39]\n" \
  "ds_read_b64_tr_b16 a[150:151], v73 offset:28672\n" \
  "v_mfma_f32_16x16x32_bf16 v[40:43], a[132:135], a[80:83], v[40:43]\n" \
  "ds_read_b64_tr_b16 a[152:153], v74 offset:24576\n" \
  "buffer_load_dwordx4 v82, s[36:39], s65 offen offset:2048 lds\n" \
  "v_mfma_f32_16x16x32_bf16 v[44:47], a[132:135], a[84:87], v[44:47]\n" \
  "ds_read_b64_tr_b16 a[154:155], v74 offset:28672\n" \
  "s_waitcnt lgkmcnt(6)\n" \
  "v_mfma_f32_16x16x32_bf16 v[48:51], a[136:139], a[80:83], v[48:51]\n" \
  "ds_read_b64_tr_b16 a[156:157], v75 offset:24576\n" \
  "buffer_load_dwordx4 a[24:27], v84, s[40:43], s66 offen offset:2048 sc1\n" \
  "v_mfma_f32_16x16x32_bf16 v[52:55], a[136:139], a[84:87], v[52:55]\n" \
  "ds_read_b64_tr_b16 a[158:159], v75 offset:28672\n" \
  "v_mfma_f32_16x16x32_bf16 v[56:59], a[140:143], a[80:83], v[56:59]\n" \
  "ds_read_b64_tr_b16 a[160:161], v76 offset:24576\n" \
  "buffer_load_dwordx4 v83, s[36:39], s65 offen offset:3072 lds\n" \
  "v_mfma_f32_16x16x32_bf16 v[60:63], a[140:143], a[84:87], v[60:63]\n" \
  "ds_read_b64_tr_b16 a[162:163], v76 offset:28672\n" \
  "s_waitcnt lgkmcnt(6)\n" \
  "v_mfma_f32_16x16x32_bf16 v[0:3], a[144:147], a[88:91], v[0:3]\n" \
  "ds_read_b64_tr_b16 a[164:165], v77 offset:24576\n" \
  "buffer_load_dwordx4 a[28:31], v84, s[40:43], s66 offen offset:3072 sc1\n" \
  "v_mfma_f32_16x16x32_bf16 v[4:7], a[144:147], a[92:95], v[4:7]\n" \
  "ds_read_b64_tr_b16 a[166:167], v77 offset:28672\n" \
  "s_add_u32 s65, s65, 131072\n" \
  "v_mfma_f32_16x16x32_bf16 v[8:11], a[148:151], a[88:91], v[8:11]\n" \
  "ds_read_b64_tr_b16 a[168:169], v78 offset:24576\n" \
  "s_add_u32 s66, s66, 4096\n" \
  "v_mfma_f32_16x16x32_bf16 v[12:15], a[148:151], a[92:95], v[12:15]\n" \
  "ds_read_b64_tr_b16 a[170:171], v78 offset:28672\n" \
  "s_waitcnt lgkmcnt(6)\n" \
  "v_mfma_f32_16x16x32_bf16 v[16:19], a[152:155], a[88:91], v[16:19]\n" \
  "ds_read_b64_tr_b16 a[172:173], v79 offset:24576\n" \
  "v_mfma_f32_16x16x32_bf16 v[20:23], a[152:155], a[92:95], v[20:23]\n" \
  "ds_read_b64_tr_b16 a[174:175], v79 offset:28672\n" \
  "s_waitcnt vmcnt(16)\n" \
  "s_barrier\n" \
  "v_mfma_f32_16x16x32_bf16 v[24:27], a[156:159], a[88:91], v[24:27]\n" \
  "ds_read_b64_tr_b16 a[112:113], v72 offset:32768\n" \
  "v_mfma_f32_16x16x32_bf16 v[28:31], a[156:159], a[92:95], v[28:31]\n" \
  "ds_read_b64_tr_b16 a[114:115], v72 offset:36864\n" \
  "s_waitcnt lgkmcnt(6)\n" \
  "v_mfma_f32_16x16x32_bf16 v[32:35], a[160:163], a[88:91], v[32:35]\n" \
  "ds_read_b64_tr_b16 a[116:117], v73 offset:32768\n" \
  "v_mfma_f32_16x16x32_bf16 v[36:39], a[160:163], a[92:95], v[36:39]\n" \
  "ds_read_b64_tr_b16 a[118:119], v73 offset:36864\n" \
  "v_mfma_f32_16x16x32_bf16 v[40:43], a[164:167], a[88:91], v[40:43]\n" \
  "ds_read_b64_tr_b16 a[120:121], v74 offset:32768\n" \
  "v_mfma_f32_16x16x32_bf16 v[44:47], a[164:167], a[92:95], v[44:47]\n" \
  "ds_read_b64_tr_b16 a[122:123], v74 offset:36864\n" \
  "s_waitcnt lgkmcnt(6)\n" \
  "v_mfma_f32_16x16x32_bf16 v[48:51], a[168:171], a[88:91], v[48:51]\n" \
  "ds_read_b64_tr_b16 a[124:125], v75 offset:32768\n" \
  "v_mfma_f32_16x16x32_bf16 v[52:55], a[168:171], a[92:95], v[52:55]\n" \
  "ds_read_b64_tr_b16 a[126:127], v75 offset:36864\n" \
  "v_mfma_f32_16x16x32_bf16 v[56:59], a[172:175], a[88:91], v[56:59]\n" \
  "ds_read_b64_tr_b16 a[128:129], v76 offset:32768\n" \
  "v_mfma_f32_16x16x32_bf16 v[60:63], a[172:175], a[92:95], v[60:63]\n" \
  "ds_read_b64_tr_b16 a[130:131], v76 offset:36864\n" \
  "s_waitcnt lgkmcnt(6)\n" \
  "v_mfma_f32_16x16x32_bf16 v[0:3], a[112:115], a[96:99], v[0:3]\n" \
  "s_add_u32 m0, s64, 32768\n" \
  "ds_read_b64_tr_b16 a[132:133], v77 offset:32768\n" \
  "v_mfma_f32_16x16x32_bf16 v[4:7], a[112:115], a[100:103], v[4:7]\n" \
  "ds_read_b64_tr_b16 a[134:135], v77 offset:36864\n" \
  "v_mfma_f32_16x16x32_bf16 v[8:11], a[116:119], a[96:99], v[8:11]\n" \
  "ds_read_b64_tr_b16 a[136:137], v78 offset:32768\n" \
  "buffer_load_dwordx4 v80, s[36:39], s65 offen offset:0 lds\n" \
  "v_mfma_f32_16x16x32_bf16 v[12:15], a[116:119], a[100:103], v[12:15]\n" \
  "ds_read_b64_tr_b16 a[138:139], v78 offset:36864\n" \
  "s_waitcnt lgkmcnt(6)\n" \
  "v_mfma_f32_16x16x32_bf16 v[16:19], a[120:123], a[96:99], v[16:19]\n" \
  "ds_read_b64_tr_b16 a[140:141], v79 offset:32768\n" \
  "buffer_load_dwordx4 a[32:35], v84, s[40:43], s66 offen offset:0 sc1\n" \
  "v_mfma_f32_16x16x32_bf16 v[20:23], a[120:123], a[100:103], v[20:23]\n" \
  "ds_read_b64_tr_b16 a[142:143], v79 offset:36864\n" \
  "v_mfma_f32_16x16x32_bf16 v[24:27], a[124:127], a[96:99], v[24:27]\n" \
  "ds_read_b64_tr_b16 a[144:145], v72 offset:40960\n" \
  "buffer_load_dwordx4 v81, s[36:39], s65 offen offset:1024 lds\n" \
  "v_mfma_f32_16x16x32_bf16 v[28:31], a[124:127], a[100:103], v[28:31]\n" \
  "ds_read_b64_tr_b16 a[146:147], v72 offset:45056\n" \
  "s_waitcnt lgkmcnt(6)\n" \
  "v_mfma_f32_16x16x32_bf16 v[32:35], a[128:131], a[96:99], v[32:35]\n" \
  "ds_read_b64_tr_b16 a[148:149], v73 offset:40960\n" \
  "buffer_load_dwordx4 a[36:39], v84, s[40:43], s66 offen offset:1024 sc1\n" \
  "v_mfma_f32_16x16x32_bf16 v[36:39], a[128:131], a[100:103], v[36:39]\n" \
  "ds_read_b64_tr_b16 a[150:151], v73 offset:45056\n" \
  "v_mfma_f32_16x16x32_bf16 v[40:43], a[132:135], a[96:99], v[40:43]\n" \
  "ds_read_b64_tr_b16 a[152:153], v74 offset:40960\n" \
  "buffer_load_dwordx4 v82, s[36:39], s65 offen offset:2048 lds\n" \
  "v_mfma_f32_16x16x32_bf16 v[44:47], a[132:135], a[100:103], v[44:47]\n" \
  "ds_read_b64_tr_b16 a[154:155], v74 offset:45056\n" \
  "s_waitcnt lgkmcnt(6)\n" \
  "v_mfma_f32_16x16x32_bf16 v[48:51], a[136:139], a[96:99], v[48:51]\n" \
  "ds_read_b64_tr_b16 a[156:157], v75 offset:40960\n" \
  "buffer_load_dwordx4 a[40:43], v84, s[40:43], s66 offen offset:2048 sc1\n" \
  "v_mfma_f32_16x16x32_bf16 v[52:55], a[136:139], a[100:103], v[52:55]\n" \
  "ds_read_b64_tr_b16 a[158:159], v75 offset:45056\n" \
  "v_mfma_f32_16x16x32_bf16 v[56:59], a[140:143], a[96:99], v[56:59]\n" \
  "ds_read_b64_tr_b16 a[160:161], v76 offset:40960\n" \
  "buffer_load_dwordx4 v83, s[36:39], s65 offen offset:3072 lds\n" \
  "v_mfma_f32_16x16x32_bf16 v[60:63], a[140:143], a[100:103], v[60:63]\n" \
  "ds_read_b64_tr_b16 a[162:163], v76 offset:45056\n" \
  "s_waitcnt lgkmcnt(6)\n" \
  "v_mfma_f32_16x16x32_bf16 v[0:3], a[144:147], a[104:107], v[0:3]\n" \
  "ds_read_b64_tr_b16 a[164:165], v77 offset:40960\n" \
  "buffer_load_dwordx4 a[44:47], v84, s[40:43], s66 offen offset:3072 sc1\n" \
  "v_mfma_f32_16x16x32_bf16 v[4:7], a[144:147], a[108:111], v[4:7]\n" \
  "ds_read_b64_tr_b16 a[166:167], v77 offset:45056\n" \
  "s_add_u32 s65, s65, 131072\n" \
  "v_mfma_f32_16x16x32_bf16 v[8:11], a[148:151], a[104:107], v[8:11]\n" \
  "ds_read_b64_tr_b16 a[168:169], v78 offset:40960\n" \
  "s_add_u32 s66, s66, 4096\n" \
  "v_mfma_f32_16x16x32_bf16 v[12:15], a[148:151], a[108:111], v[12:15]\n" \
  "ds_read_b64_tr_b16 a[170:171], v78 offset:45056\n" \
  "s_waitcnt lgkmcnt(6)\n" \
  "v_mfma_f32_16x16x32_bf16 v[16:19], a[152:155], a[104:107], v[16:19]\n" \
  "ds_read_b64_tr_b16 a[172:173], v79 offset:40960\n" \
  "v_mfma_f32_16x16x32_bf16 v[20:23], a[152:155], a[108:111], v[20:23]\n" \
  "ds_read_b64_tr_b16 a[174:175], v79 offset:45056\n" \
  "s_waitcnt vmcnt(16)\n" \
  "s_barrier\n" \
  "v_mfma_f32_16x16x32_bf16 v[24:27], a[156:159], a[104:107], v[24:27]\n" \
  "ds_read_b64_tr_b16 a[112:113], v64 offset:0\n" \
  "v_mfma_f32_16x16x32_bf16 v[28:31], a[156:159], a[108:111], v[28:31]\n" \
  "ds_read_b64_tr_b16 a[114:115], v64 offset:4096\n" \
  "s_waitcnt lgkmcnt(6)\n" \
  "v_mfma_f32_16x16x32_bf16 v[32:35], a[160:163], a[104:107], v[32:35]\n" \
  "ds_read_b64_tr_b16 a[116:117], v65 offset:0\n" \
  "v_mfma_f32_16x16x32_bf16 v[36:39], a[160:163], a[108:111], v[36:39]\n" \
  "ds_read_b64_tr_b16 a[118:119], v65 offset:4096\n" \
  "v_mfma_f32_16x16x32_bf16 v[40:43], a[164:167], a[104:107], v[40:43]\n" \
  "ds_read_b64_tr_b16 a[120:121], v66 offset:0\n" \
  "v_mfma_f32_16x16x32_bf16 v[44:47], a[164:167], a[108:111], v[44:47]\n" \
  "ds_read_b64_tr_b16 a[122:123], v66 offset:4096\n" \
  "s_waitcnt lgkmcnt(6)\n" \
  "v_mfma_f32_16x16x32_bf16 v[48:51], a[168:171], a[104:107], v[48:51]\n" \
  "ds_read_b64_tr_b16 a[124:125], v67 offset:0\n" \
  "v_mfma_f32_16x16x32_bf16 v[52:55], a[168:171], a[108:111], v[52:55]\n" \
  "ds_read_b64_tr_b16 a[126:127], v67 offset:4096\n" \
  "s_add_u32 s67, s67, 1\n" \
  "v_mfma_f32_16x16x32_bf16 v[56:59], a[172:175], a[104:107], v[56:59]\n" \
  "ds_read_b64_tr_b16 a[128:129], v68 offset:0\n" \
  "s_cmp_lt_u32 s67, 4\n" \
  "v_mfma_f32_16x16x32_bf16 v[60:63], a[172:175], a[108:111], v[60:63]\n" \
  "ds_read_b64_tr_b16 a[130:131], v68 offset:4096\n" \
  "s_cbranch_scc1 L_G2_loop_%=\n" \
  "s_add_u32 s75, s72, 32768\n" \
  "buffer_load_dwordx4 v[128:131], v84, s[52:55], s72 offen offset:0 sc1\n" \
  "buffer_load_dwordx4 v[132:135], v84, s[52:55], s72 offen offset:1024 sc1\n" \
  "buffer_load_dwordx4 v[136:139], v84, s[52:55], s75 offen offset:0 sc1\n" \
  "buffer_load_dwordx4 v[140:143], v84, s[52:55], s75 offen offset:1024 sc1\n" \
  "buffer_load_dwordx4 v[144:147], v85, s[56:59], s73 offen offset:0\n" \
  "buffer_load_dwordx4 v[148:151], v85, s[56:59], s73 offen offset:64\n" \
  "buffer_load_dwordx4 v[152:155], v85, s[56:59], s73 offen offset:128\n" \
  "buffer_load_dwordx4 v[156:159], v85, s[56:59], s73 offen offset:192\n" \
  "s_nop 7\n" \
  "s_add_u32 s76, s68, 4096\n" \
  "buffer_store_dwordx4 v[0:3], v84, s[44:47], s68 offen offset:0 sc1\n" \
  "buffer_store_dwordx4 v[4:7], v84, s[44:47], s68 offen offset:1024 sc1\n" \
  "buffer_store_dwordx4 v[8:11], v84, s[44:47], s68 offen offset:2048 sc1\n" \
  "buffer_store_dwordx4 v[12:15], v84, s[44:47], s68 offen offset:3072 sc1\n" \
  "buffer_store_dwordx4 v[16:19], v84, s[44:47], s76 offen offset:0 sc1\n" \
  "buffer_store_dwordx4 v[20:23], v84, s[44:47], s76 offen offset:1024 sc1\n" \
  "buffer_store_dwordx4 v[24:27], v84, s[44:47], s76 offen offset:2048 sc1\n" \
  "buffer_store_dwordx4 v[28:31], v84, s[44:47], s76 offen offset:3072 sc1\n" \
  "s_waitcnt vmcnt(0)\n" \
  "s_barrier\n" \
  "s_cmp_lg_u32 s74, 0\n" \
  "s_cbranch_scc1 L_G2_nopoll_%=\n" \
  "v_mov_b32 v160, 1\n" \
  "v_mov_b32 v161, 0\n" \
  "s_mov_b64 exec, 1\n" \
  "buffer_store_dword v160, v161, s[60:63], s70 offen sc1\n" \
  "s_mov_b32 s77, 0\n" \
  "L_G2_poll_%=:\n" \
  "buffer_load_dword v162, v161, s[60:63], s71 offen sc1\n" \
  "s_add_u32 s77, s77, 1\n" \
  "s_waitcnt vmcnt(0)\n" \
  "v_readfirstlane_b32 s78, v162\n" \
  "s_cmp_ge_u32 s77, 0x100000\n" \
  "s_cbranch_scc1 L_G2_pollend_%=\n" \
  "s_cmp_eq_u32 s78, 1\n" \
  "s_cbranch_scc1 L_G2_pollend_%=\n" \
  "s_sleep 1\n" \
  "s_branch L_G2_poll_%=\n" \
  "L_G2_pollend_%=:\n" \
  "s_mov_b64 exec, -1\n" \
  "L_G2_nopoll_%=:\n" \
  "s_barrier\n" \
  "s_add_u32 s76, s69, 4096\n" \
  "buffer_load_dwordx4 v[96:99], v84, s[44:47], s69 offen offset:0 sc1\n" \
  "buffer_load_dwordx4 v[100:103], v84, s[44:47], s69 offen offset:1024 sc1\n" \
  "buffer_load_dwordx4 v[104:107], v84, s[44:47], s69 offen offset:2048 sc1\n" \
  "buffer_load_dwordx4 v[108:111], v84, s[44:47], s69 offen offset:3072 sc1\n" \
  "buffer_load_dwordx4 v[112:115], v84, s[44:47], s76 offen offset:0 sc1\n" \
  "buffer_load_dwordx4 v[116:119], v84, s[44:47], s76 offen offset:1024 sc1\n" \
  "buffer_load_dwordx4 v[120:123], v84, s[44:47], s76 offen offset:2048 sc1\n" \
  "buffer_load_dwordx4 v[124:127], v84, s[44:47], s76 offen offset:3072 sc1\n" \
  "s_waitcnt vmcnt(0)\n" \
  "v_pk_add_f32 v[32:33], v[32:33], v[96:97]\n" \
  "v_pk_add_f32 v[34:35], v[34:35], v[98:99]\n" \
  "v_lshlrev_b32 v160, 16, v128\n" \
  "v_and_b32 v161, 0xffff0000, v128\n" \
  "v_lshlrev_b32 v162, 16, v129\n" \
  "v_and_b32 v163, 0xffff0000, v129\n" \
  "v_pk_fma_f32 v[160:161], v[32:33], v[144:145], v[160:161]\n" \
  "v_pk_fma_f32 v[162:163], v[34:35], v[146:147], v[162:163]\n" \
  "v_cvt_pk_bf16_f32 v168, v160, v161\n" \
  "v_cvt_pk_bf16_f32 v169, v162, v163\n" \
  "v_pk_add_f32 v[40:41], v[40:41], v[104:105]\n" \
  "v_pk_add_f32 v[42:43], v[42:43], v[106:107]\n" \
  "v_lshlrev_b32 v160, 16, v130\n" \
  "v_and_b32 v161, 0xffff0000, v130\n" \
  "v_lshlrev_b32 v162, 16, v131\n" \
  "v_and_b32 v163, 0xffff0000, v131\n" \
  "v_pk_fma_f32 v[160:161], v[40:41], v[148:149], v[160:161]\n" \
  "v_pk_fma_f32 v[162:163], v[42:43], v[150:151], v[162:163]\n" \
  "v_cvt_pk_bf16_f32 v170, v160, v161\n" \
  "v_cvt_pk_bf16_f32 v171, v162, v163\n" \
  "s_nop 1\n" \
  "v_permlane16_swap_b32 v168, v170\n" \
  "v_permlane16_swap_b32 v169, v171\n" \
  "s_nop 1\n" \
  "buffer_store_dwordx4 v[168:171], v86, s[48:51], s96 offen offset:0\n" \
  "v_pk_add_f32 v[36:37], v[36:37], v[100:101]\n" \
  "v_pk_add_f32 v[38:39], v[38:39], v[102:103]\n" \
  "v_lshlrev_b32 v160, 16, v136\n" \
  "v_and_b32 v161, 0xffff0000, v136\n" \
  "v_lshlrev_b32 v162, 16, v137\n" \
  "v_and_b32 v163, 0xffff0000, v137\n" \
  "v_pk_fma_f32 v[160:161], v[36:37], v[144:145], v[160:161]\n" \
  "v_pk_fma_f32 v[162:163], v[38:39], v[146:147], v[162:163]\n" \
  "v_cvt_pk_bf16_f32 v172, v160, v161\n" \
  "v_cvt_pk_bf16_f32 v173, v162, v163\n" \
  "v_pk_add_f32 v[44:45], v[44:45], v[108:109]\n" \
  "v_pk_add_f32 v[46:47], v[46:47], v[110:111]\n" \
  "v_lshlrev_b32 v160, 16, v138\n" \
  "v_and_b32 v161, 0xffff0000, v138\n" \
  "v_lshlrev_b32 v162, 16, v139\n" \
  "v_and_b32 v163, 0xffff0000, v139\n" \
  "v_pk_fma_f32 v[160:161], v[44:45], v[148:149], v[160:161]\n" \
  "v_pk_fma_f32 v[162:163], v[46:47], v[150:151], v[162:163]\n" \
  "v_cvt_pk_bf16_f32 v174, v160, v161\n" \
  "v_cvt_pk_bf16_f32 v175, v162, v163\n" \
  "s_nop 1\n" \
  "v_permlane16_swap_b32 v172, v174\n" \
  "v_permlane16_swap_b32 v173, v175\n" \
  "s_nop 1\n" \
  "buffer_store_dwordx4 v[172:175], v86, s[48:51], s97 offen offset:0\n" \
  "v_pk_add_f32 v[48:49], v[48:49], v[112:113]\n" \
  "v_pk_add_f32 v[50:51], v[50:51], v[114:115]\n" \
  "v_lshlrev_b32 v160, 16, v132\n" \
  "v_and_b32 v161, 0xffff0000, v132\n" \
  "v_lshlrev_b32 v162, 16, v133\n" \
  "v_and_b32 v163, 0xffff0000, v133\n" \
  "v_pk_fma_f32 v[160:161], v[48:49], v[152:153], v[160:161]\n" \
  "v_pk_fma_f32 v[162:163], v[50:51], v[154:155], v[162:163]\n" \
  "v_cvt_pk_bf16_f32 v176, v160, v161\n" \
  "v_cvt_pk_bf16_f32 v177, v162, v163\n" \
  "v_pk_add_f32 v[56:57], v[56:57], v[120:121]\n" \
  "v_pk_add_f32 v[58:59], v[58:59], v[122:123]\n" \
  "v_lshlrev_b32 v160, 16, v134\n" \
  "v_and_b32 v161, 0xffff0000, v134\n" \
  "v_lshlrev_b32 v162, 16, v135\n" \
  "v_and_b32 v163, 0xffff0000, v135\n" \
  "v_pk_fma_f32 v[160:161], v[56:57], v[156:157], v[160:161]\n" \
  "v_pk_fma_f32 v[162:163], v[58:59], v[158:159], v[162:163]\n" \
  "v_cvt_pk_bf16_f32 v178, v160, v161\n" \
  "v_cvt_pk_bf16_f32 v179, v162, v163\n" \
  "s_nop 1\n" \
  "v_permlane16_swap_b32 v176, v178\n" \
  "v_permlane16_swap_b32 v177, v179\n" \
  "s_nop 1\n" \
  "buffer_store_dwordx4 v[176:179], v86, s[48:51], s96 offen offset:64\n" \
  "v_pk_add_f32 v[52:53], v[52:53], v[116:117]\n" \
  "v_pk_add_f32 v[54:55], v[54:55], v[118:119]\n" \
  "v_lshlrev_b32 v160, 16, v140\n" \
  "v_and_b32 v161, 0xffff0000, v140\n" \
  "v_lshlrev_b32 v162, 16, v141\n" \
  "v_and_b32 v163, 0xffff0000, v141\n" \
  "v_pk_fma_f32 v[160:161], v[52:53], v[152:153], v[160:161]\n" \
  "v_pk_fma_f32 v[162:163], v[54:55], v[154:155], v[162:163]\n" \
  "v_cvt_pk_bf16_f32 v180, v160, v161\n" \
  "v_cvt_pk_bf16_f32 v181, v162, v163\n" \
  "v_pk_add_f32 v[60:61], v[60:61], v[124:125]\n" \
  "v_pk_add_f32 v[62:63], v[62:63], v[126:127]\n" \
  "v_lshlrev_b32 v160, 16, v142\n" \
  "v_and_b32 v161, 0xffff0000, v142\n" \
  "v_lshlrev_b32 v162, 16, v143\n" \
  "v_and_b32 v163, 0xffff0000, v143\n" \
  "v_pk_fma_f32 v[160:161], v[60:61], v[156:157], v[160:161]\n" \
  "v_pk_fma_f32 v[162:163], v[62:63], v[158:159], v[162:163]\n" \
  "v_cvt_pk_bf16_f32 v182, v160, v161\n" \
  "v_cvt_pk_bf16_f32 v183, v162, v163\n" \
  "s_nop 1\n" \
  "v_permlane16_swap_b32 v180, v182\n" \
  "v_permlane16_swap_b32 v181, v183\n" \
  "s_nop 1\n" \
  "buffer_store_dwordx4 v[180:183], v86, s[48:51], s97 offen offset:64\n" \
  "s_waitcnt vmcnt(0) lgkmcnt(0)\n" \
  ""
#define K2_ASM \
  "buffer_load_dwordx4 v[32:35], v107, s[36:39], s45 offen offset:0 sc1\n" \
  "buffer_load_dwordx4 v[36:39], v107, s[36:39], s45 offen offset:1024 sc1\n" \
  "buffer_load_dwordx4 v[40:43], v107, s[36:39], s45 offen offset:2048 sc1\n" \
  "buffer_load_dwordx4 v[44:47], v107, s[36:39], s45 offen offset:3072 sc1\n" \
  "v_mov_b32 v0, 0\n" \
  "v_mov_b32 v1, 0\n" \
  "v_mov_b32 v2, 0\n" \
  "v_mov_b32 v3, 0\n" \
  "v_mov_b32 v4, 0\n" \
  "v_mov_b32 v5, 0\n" \
  "v_mov_b32 v6, 0\n" \
  "v_mov_b32 v7, 0\n" \
  "v_mov_b32 v8, 0\n" \
  "v_mov_b32 v9, 0\n" \
  "v_mov_b32 v10, 0\n" \
  "v_mov_b32 v11, 0\n" \
  "v_mov_b32 v12, 0\n" \
  "v_mov_b32 v13, 0\n" \
  "v_mov_b32 v14, 0\n" \
  "v_mov_b32 v15, 0\n" \
  "v_mov_b32 v16, 0\n" \
  "v_mov_b32 v17, 0\n" \
  "v_mov_b32 v18, 0\n" \
  "v_mov_b32 v19, 0\n" \
  "v_mov_b32 v20, 0\n" \
  "v_mov_b32 v21, 0\n" \
  "v_mov_b32 v22, 0\n" \
  "v_mov_b32 v23, 0\n" \
  "v_mov_b32 v24, 0\n" \
  "v_mov_b32 v25, 0\n" \
  "v_mov_b32 v26, 0\n" \
  "v_mov_b32 v27, 0\n" \
  "v_mov_b32 v28, 0\n" \
  "v_mov_b32 v29, 0\n" \
  "v_mov_b32 v30, 0\n" \
  "v_mov_b32 v31, 0\n" \
  "s_waitcnt vmcnt(0)\n" \
  "ds_write_b128 v145, v[32:35] offset:0\n" \
  "ds_write_b128 v145, v[36:39] offset:1024\n" \
  "ds_write_b128 v145, v[40:43] offset:2048\n" \
  "ds_write_b128 v145, v[44:47] offset:3072\n" \
  "s_waitcnt lgkmcnt(0)\n" \
  "ds_read_b128 a[28:31], v104 offset:448\n" \
  "ds_read_b128 a[60:63], v104 offset:960\n" \
  "ds_read_b128 a[92:95], v104 offset:1472\n" \
  "ds_read_b128 a[124:127], v104 offset:1984\n" \
  "ds_read_b128 a[156:159], v104 offset:2496\n" \
  "ds_read_b128 a[188:191], v104 offset:3008\n" \
  "ds_read_b128 a[220:223], v104 offset:3520\n" \
  "ds_read_b128 a[252:255], v104 offset:4032\n" \
  "ds_read_b128 a[24:27], v104 offset:384\n" \
  "ds_read_b128 a[56:59], v104 offset:896\n" \
  "ds_read_b128 a[88:91], v104 offset:1408\n" \
  "ds_read_b128 a[120:123], v104 offset:1920\n" \
  "ds_read_b128 a[152:155], v104 offset:2432\n" \
  "ds_read_b128 a[184:187], v104 offset:2944\n" \
  "ds_read_b128 a[216:219], v104 offset:3456\n" \
  "ds_read_b128 a[248:251], v104 offset:3968\n" \
  "ds_read_b128 a[20:23], v104 offset:320\n" \
  "ds_read_b128 a[52:55], v104 offset:832\n" \
  "ds_read_b128 a[84:87], v104 offset:1344\n" \
  "ds_read_b128 a[116:119], v104 offset:1856\n" \
  "ds_read_b128 a[148:151], v104 offset:2368\n" \
  "ds_read_b128 a[180:183], v104 offset:2880\n" \
  "ds_read_b128 a[212:215], v104 offset:3392\n" \
  "ds_read_b128 a[244:247], v104 offset:3904\n" \
  "ds_read_b128 a[16:19], v104 offset:256\n" \
  "ds_read_b128 a[48:51], v104 offset:768\n" \
  "ds_read_b128 a[80:83], v104 offset:1280\n" \
  "ds_read_b128 a[112:115], v104 offset:1792\n" \
  "ds_read_b128 a[144:147], v104 offset:2304\n" \
  "ds_read_b128 a[176:179], v104 offset:2816\n" \
  "ds_read_b128 a[208:211], v104 offset:3328\n" \
  "ds_read_b128 a[240:243], v104 offset:3840\n" \
  "ds_read_b128 a[12:15], v104 offset:192\n" \
  "ds_read_b128 a[44:47], v104 offset:704\n" \
  "ds_read_b128 a[76:79], v104 offset:1216\n" \
  "ds_read_b128 a[108:111], v104 offset:1728\n" \
  "ds_read_b128 a[140:143], v104 offset:2240\n" \
  "ds_read_b128 a[172:175], v104 offset:2752\n" \
  "ds_read_b128 a[204:207], v104 offset:3264\n" \
  "ds_read_b128 a[236:239], v104 offset:3776\n" \
  "ds_read_b128 a[8:11], v104 offset:128\n" \
  "ds_read_b128 a[40:43], v104 offset:640\n" \
  "ds_read_b128 a[72:75], v104 offset:1152\n" \
  "ds_read_b128 a[104:107], v104 offset:1664\n" \
  "ds_read_b128 a[136:139], v104 offset:2176\n" \
  "ds_read_b128 a[168:171], v104 offset:2688\n" \
  "ds_read_b128 a[200:203], v104 offset:3200\n" \
  "ds_read_b128 a[232:235], v104 offset:3712\n" \
  "ds_read_b128 a[4:7], v104 offset:64\n" \
  "ds_read_b128 a[36:39], v104 offset:576\n" \
  "ds_read_b128 a[68:71], v104 offset:1088\n" \
  "ds_read_b128 a[100:103], v104 offset:1600\n" \
  "ds_read_b128 a[132:135], v104 offset:2112\n" \
  "ds_read_b128 a[164:167], v104 offset:2624\n" \
  "ds_read_b128 a[196:199], v104 offset:3136\n" \
  "ds_read_b128 a[228:231], v104 offset:3648\n" \
  "ds_read_b128 a[0:3], v104 offset:0\n" \
  "ds_read_b128 a[32:35], v104 offset:512\n" \
  "ds_read_b128 a[64:67], v104 offset:1024\n" \
  "ds_read_b128 a[96:99], v104 offset:1536\n" \
  "ds_read_b128 a[128:131], v104 offset:2048\n" \
  "ds_read_b128 a[160:163], v104 offset:2560\n" \
  "ds_read_b128 a[192:195], v104 offset:3072\n" \
  "ds_read_b128 a[224:227], v104 offset:3584\n" \
  "v_and_b32 v147, 248, v108\n" \
  "v_lshlrev_b32 v147, 4, v147\n" \
  "s_mov_b32 s92, 0\n" \
  "buffer_load_dword v146, v147, s[76:79], s52 offen offset:0\n" \
  "ds_read2_b32 v[32:33], v96 offset0:0 offset1:2\n" \
  "ds_read2_b32 v[34:35], v96 offset0:4 offset1:6\n" \
  "ds_read2_b32 v[36:37], v96 offset0:32 offset1:34\n" \
  "ds_read2_b32 v[38:39], v96 offset0:36 offset1:38\n" \
  "ds_read2_b32 v[40:41], v96 offset0:64 offset1:66\n" \
  "ds_read2_b32 v[42:43], v96 offset0:68 offset1:70\n" \
  "ds_read2_b32 v[44:45], v96 offset0:96 offset1:98\n" \
  "ds_read2_b32 v[46:47], v96 offset0:100 offset1:102\n" \
  "ds_read2_b32 v[48:49], v96 offset0:128 offset1:130\n" \
  "ds_read2_b32 v[50:51], v96 offset0:132 offset1:134\n" \
  "ds_read2_b32 v[52:53], v96 offset0:160 offset1:162\n" \
  "ds_read2_b32 v[54:55], v96 offset0:164 offset1:166\n" \
  "s_waitcnt lgkmcnt(10)\n" \
  "v_mfma_f32_16x16x32_bf16 v[0:3], v[32:35], a[28:31], v[0:3]\n" \
  "ds_read2_b32 v[56:57], v96 offset0:192 offset1:194\n" \
  "v_mfma_f32_16x16x32_bf16 v[4:7], v[32:35], a[60:63], v[4:7]\n" \
  "buffer_load_dwordx4 v[160:163], v149, s[88:91], s92 offen\n" \
  "s_add_u32 s92, s92, 12288\n" \
  "ds_read2_b32 v[58:59], v96 offset0:196 offset1:198\n" \
  "v_mfma_f32_16x16x32_bf16 v[8:11], v[32:35], a[92:95], v[8:11]\n" \
  "ds_read2_b32 v[60:61], v96 offset0:224 offset1:226\n" \
  "v_mfma_f32_16x16x32_bf16 v[12:15], v[32:35], a[124:127], v[12:15]\n" \
  "buffer_load_dword v146, v147, s[80:83], s52 offen offset:0\n" \
  "ds_read2_b32 v[62:63], v96 offset0:228 offset1:230\n" \
  "v_mfma_f32_16x16x32_bf16 v[16:19], v[32:35], a[156:159], v[16:19]\n" \
  "ds_read2_b32 v[64:65], v97 offset0:0 offset1:2\n" \
  "v_mfma_f32_16x16x32_bf16 v[20:23], v[32:35], a[188:191], v[20:23]\n" \
  "ds_read2_b32 v[66:67], v97 offset0:4 offset1:6\n" \
  "v_mfma_f32_16x16x32_bf16 v[24:27], v[32:35], a[220:223], v[24:27]\n" \
  "ds_read2_b32 v[68:69], v97 offset0:32 offset1:34\n" \
  "v_mfma_f32_16x16x32_bf16 v[28:31], v[32:35], a[252:255], v[28:31]\n" \
  "ds_read2_b32 v[70:71], v97 offset0:36 offset1:38\n" \
  "s_waitcnt lgkmcnt(15)\n" \
  "v_mfma_f32_16x16x32_bf16 v[0:3], v[36:39], a[24:27], v[0:3]\n" \
  "ds_read2_b32 v[72:73], v97 offset0:64 offset1:66\n" \
  "v_mfma_f32_16x16x32_bf16 v[4:7], v[36:39], a[56:59], v[4:7]\n" \
  "ds_read2_b32 v[74:75], v97 offset0:68 offset1:70\n" \
  "v_mfma_f32_16x16x32_bf16 v[8:11], v[36:39], a[88:91], v[8:11]\n" \
  "ds_read2_b32 v[76:77], v97 offset0:96 offset1:98\n" \
  "v_mfma_f32_16x16x32_bf16 v[12:15], v[36:39], a[120:123], v[12:15]\n" \
  "ds_read2_b32 v[78:79], v97 offset0:100 offset1:102\n" \
  "v_mfma_f32_16x16x32_bf16 v[16:19], v[36:39], a[152:155], v[16:19]\n" \
  "ds_read2_b32 v[80:81], v97 offset0:128 offset1:130\n" \
  "v_mfma_f32_16x16x32_bf16 v[20:23], v[36:39], a[184:187], v[20:23]\n" \
  "ds_read2_b32 v[82:83], v97 offset0:132 offset1:134\n" \
  "v_mfma_f32_16x16x32_bf16 v[24:27], v[36:39], a[216:219], v[24:27]\n" \
  "ds_read2_b32 v[84:85], v97 offset0:160 offset1:162\n" \
  "v_mfma_f32_16x16x32_bf16 v[28:31], v[36:39], a[248:251], v[28:31]\n" \
  "ds_read2_b32 v[86:87], v97 offset0:164 offset1:166\n" \
  "s_waitcnt lgkmcnt(15)\n" \
  "v_mfma_f32_16x16x32_bf16 v[0:3], v[40:43], a[20:23], v[0:3]\n" \
  "ds_read2_b32 v[88:89], v97 offset0:192 offset1:194\n" \
  "v_mfma_f32_16x16x32_bf16 v[4:7], v[40:43], a[52:55], v[4:7]\n" \
  "buffer_load_dwordx4 v[164:167], v149, s[88:91], s92 offen\n" \
  "s_add_u32 s92, s92, 12288\n" \
  "ds_read2_b32 v[90:91], v97 offset0:196 offset1:198\n" \
  "v_mfma_f32_16x16x32_bf16 v[8:11], v[40:43], a[84:87], v[8:11]\n" \
  "ds_read2_b32 v[92:93], v97 offset0:224 offset1:226\n" \
  "v_mfma_f32_16x16x32_bf16 v[12:15], v[40:43], a[116:119], v[12:15]\n" \
  "ds_read2_b32 v[94:95], v97 offset0:228 offset1:230\n" \
  "v_mfma_f32_16x16x32_bf16 v[16:19], v[40:43], a[148:151], v[16:19]\n" \
  "ds_read2_b32 v[32:33], v98 offset0:0 offset1:2\n" \
  "v_mfma_f32_16x16x32_bf16 v[20:23], v[40:43], a[180:183], v[20:23]\n" \
  "ds_read2_b32 v[34:35], v98 offset0:4 offset1:6\n" \
  "v_mfma_f32_16x16x32_bf16 v[24:27], v[40:43], a[212:215], v[24:27]\n" \
  "ds_read2_b32 v[36:37], v98 offset0:32 offset1:34\n" \
  "v_mfma_f32_16x16x32_bf16 v[28:31], v[40:43], a[244:247], v[28:31]\n" \
  "ds_read2_b32 v[38:39], v98 offset0:36 offset1:38\n" \
  "s_waitcnt lgkmcnt(15)\n" \
  "v_mfma_f32_16x16x32_bf16 v[0:3], v[44:47], a[16:19], v[0:3]\n" \
  "ds_read2_b32 v[40:41], v98 offset0:64 offset1:66\n" \
  "v_mfma_f32_16x16x32_bf16 v[4:7], v[44:47], a[48:51], v[4:7]\n" \
  "ds_read2_b32 v[42:43], v98 offset0:68 offset1:70\n" \
  "v_mfma_f32_16x16x32_bf16 v[8:11], v[44:47], a[80:83], v[8:11]\n" \
  "v_mfma_f32_16x16x32_bf16 v[12:15], v[44:47], a[112:115], v[12:15]\n" \
  "v_mfma_f32_16x16x32_bf16 v[16:19], v[44:47], a[144:147], v[16:19]\n" \
  "v_mfma_f32_16x16x32_bf16 v[20:23], v[44:47], a[176:179], v[20:23]\n" \
  "v_mfma_f32_16x16x32_bf16 v[24:27], v[44:47], a[208:211], v[24:27]\n" \
  "v_mfma_f32_16x16x32_bf16 v[28:31], v[44:47], a[240:243], v[28:31]\n" \
  "ds_read2_b32 v[44:45], v98 offset0:96 offset1:98\n" \
  "s_waitcnt lgkmcnt(15)\n" \
  "v_mfma_f32_16x16x32_bf16 v[0:3], v[48:51], a[12:15], v[0:3]\n" \
  "ds_read2_b32 v[46:47], v98 offset0:100 offset1:102\n" \
  "v_mfma_f32_16x16x32_bf16 v[4:7], v[48:51], a[44:47], v[4:7]\n" \
  "buffer_load_dwordx4 v[168:171], v149, s[88:91], s92 offen\n" \
  "s_add_u32 s92, s92, 12288\n" \
  "v_mfma_f32_16x16x32_bf16 v[8:11], v[48:51], a[76:79], v[8:11]\n" \
  "v_mfma_f32_16x16x32_bf16 v[12:15], v[48:51], a[108:111], v[12:15]\n" \
  "v_mfma_f32_16x16x32_bf16 v[16:19], v[48:51], a[140:143], v[16:19]\n" \
  "v_mfma_f32_16x16x32_bf16 v[20:23], v[48:51], a[172:175], v[20:23]\n" \
  "v_mfma_f32_16x16x32_bf16 v[24:27], v[48:51], a[204:207], v[24:27]\n" \
  "v_mfma_f32_16x16x32_bf16 v[28:31], v[48:51], a[236:239], v[28:31]\n" \
  "ds_read2_b32 v[48:49], v98 offset0:128 offset1:130\n" \
  "s_waitcnt lgkmcnt(15)\n" \
  "v_mfma_f32_16x16x32_bf16 v[0:3], v[52:55], a[8:11], v[0:3]\n" \
  "ds_read2_b32 v[50:51], v98 offset0:132 offset1:134\n" \
  "v_mfma_f32_16x16x32_bf16 v[4:7], v[52:55], a[40:43], v[4:7]\n" \
  "v_mfma_f32_16x16x32_bf16 v[8:11], v[52:55], a[72:75], v[8:11]\n" \
  "v_mfma_f32_16x16x32_bf16 v[12:15], v[52:55], a[104:107], v[12:15]\n" \
  "v_mfma_f32_16x16x32_bf16 v[16:19], v[52:55], a[136:139], v[16:19]\n" \
  "v_mfma_f32_16x16x32_bf16 v[20:23], v[52:55], a[168:171], v[20:23]\n" \
  "v_mfma_f32_16x16x32_bf16 v[24:27], v[52:55], a[200:203], v[24:27]\n" \
  "v_mfma_f32_16x16x32_bf16 v[28:31], v[52:55], a[232:235], v[28:31]\n" \
  "ds_read2_b32 v[52:53], v98 offset0:160 offset1:162\n" \
  "s_waitcnt lgkmcnt(15)\n" \
  "v_mfma_f32_16x16x32_bf16 v[0:3], v[56:59], a[4:7], v[0:3]\n" \
  "ds_read2_b32 v[54:55], v98 offset0:164 offset1:166\n" \
  "v_mfma_f32_16x16x32_bf16 v[4:7], v[56:59], a[36:39], v[4:7]\n" \
  "buffer_load_dwordx4 v[172:175], v149, s[88:91], s92 offen\n" \
  "s_add_u32 s92, s92, 12288\n" \
  "v_mfma_f32_16x16x32_bf16 v[8:11], v[56:59], a[68:71], v[8:11]\n" \
  "v_mfma_f32_16x16x32_bf16 v[12:15], v[56:59], a[100:103], v[12:15]\n" \
  "v_mfma_f32_16x16x32_bf16 v[16:19], v[56:59], a[132:135], v[16:19]\n" \
  "v_mfma_f32_16x16x32_bf16 v[20:23], v[56:59], a[164:167], v[20:23]\n" \
  "v_mfma_f32_16x16x32_bf16 v[24:27], v[56:59], a[196:199], v[24:27]\n" \
  "v_mfma_f32_16x16x32_bf16 v[28:31], v[56:59], a[228:231], v[28:31]\n" \
  "ds_read2_b32 v[56:57], v98 offset0:192 offset1:194\n" \
  "s_waitcnt lgkmcnt(15)\n" \
  "v_mfma_f32_16x16x32_bf16 v[0:3], v[60:63], a[0:3], v[0:3]\n" \
  "ds_read2_b32 v[58:59], v98 offset0:196 offset1:198\n" \
  "v_mfma_f32_16x16x32_bf16 v[4:7], v[60:63], a[32:35], v[4:7]\n" \
  "v_mfma_f32_16x16x32_bf16 v[8:11], v[60:63], a[64:67], v[8:11]\n" \
  "v_mfma_f32_16x16x32_bf16 v[12:15], v[60:63], a[96:99], v[12:15]\n" \
  "v_mfma_f32_16x16x32_bf16 v[16:19], v[60:63], a[128:131], v[16:19]\n" \
  "v_mfma_f32_16x16x32_bf16 v[20:23], v[60:63], a[160:163], v[20:23]\n" \
  "v_mfma_f32_16x16x32_bf16 v[24:27], v[60:63], a[192:195], v[24:27]\n" \
  "v_mfma_f32_16x16x32_bf16 v[28:31], v[60:63], a[224:227], v[28:31]\n" \
  "ds_read2_b32 v[60:61], v98 offset0:224 offset1:226\n" \
  "s_waitcnt lgkmcnt(15)\n" \
  "v_mfma_f32_16x16x32_bf16 v[4:7], v[64:67], a[28:31], v[4:7]\n" \
  "ds_read2_b32 v[62:63], v98 offset0:228 offset1:230\n" \
  "v_mfma_f32_16x16x32_bf16 v[8:11], v[64:67], a[60:63], v[8:11]\n" \
  "buffer_load_dwordx4 v[176:179], v149, s[88:91], s92 offen\n" \
  "s_add_u32 s92, s92, 12288\n" \
  "ds_read_b64 v[112:113], v105 offset:0\n" \
  "v_mfma_f32_16x16x32_bf16 v[12:15], v[64:67], a[92:95], v[12:15]\n" \
  "v_mfma_f32_16x16x32_bf16 v[16:19], v[64:67], a[124:127], v[16:19]\n" \
  "v_mfma_f32_16x16x32_bf16 v[20:23], v[64:67], a[156:159], v[20:23]\n" \
  "v_mfma_f32_16x16x32_bf16 v[24:27], v[64:67], a[188:191], v[24:27]\n" \
  "v_mfma_f32_16x16x32_bf16 v[28:31], v[64:67], a[220:223], v[28:31]\n" \
  "ds_read2_b32 v[64:65], v99 offset0:0 offset1:2\n" \
  "s_waitcnt lgkmcnt(15)\n" \
  "v_mfma_f32_16x16x32_bf16 v[4:7], v[68:71], a[24:27], v[4:7]\n" \
  "ds_read2_b32 v[66:67], v99 offset0:4 offset1:6\n" \
  "s_waitcnt lgkmcnt(2)\n" \
  "v_lshlrev_b32 v131, 16, v112\n" \
  "v_mfma_f32_16x16x32_bf16 v[8:11], v[68:71], a[56:59], v[8:11]\n" \
  "v_and_b32 v130, 0xffff0000, v112\n" \
  "v_mfma_f32_16x16x32_bf16 v[12:15], v[68:71], a[88:91], v[12:15]\n" \
  "v_lshlrev_b32 v129, 16, v113\n" \
  "v_mfma_f32_16x16x32_bf16 v[16:19], v[68:71], a[120:123], v[16:19]\n" \
  "v_and_b32 v128, 0xffff0000, v113\n" \
  "v_mfma_f32_16x16x32_bf16 v[20:23], v[68:71], a[152:155], v[20:23]\n" \
  "v_fma_f32 v128, v128, s46, v0\n" \
  "v_mfma_f32_16x16x32_bf16 v[24:27], v[68:71], a[184:187], v[24:27]\n" \
  "v_fma_f32 v129, v129, s46, v1\n" \
  "v_mfma_f32_16x16x32_bf16 v[28:31], v[68:71], a[216:219], v[28:31]\n" \
  "ds_read2_b32 v[68:69], v99 offset0:32 offset1:34\n" \
  "v_fma_f32 v130, v130, s46, v2\n" \
  "s_waitcnt lgkmcnt(15)\n" \
  "v_mfma_f32_16x16x32_bf16 v[4:7], v[72:75], a[20:23], v[4:7]\n" \
  "ds_read2_b32 v[70:71], v99 offset0:36 offset1:38\n" \
  "v_fma_f32 v131, v131, s46, v3\n" \
  "v_mfma_f32_16x16x32_bf16 v[8:11], v[72:75], a[52:55], v[8:11]\n" \
  "v_cvt_pk_bf16_f32 v132, v128, v129\n" \
  "v_mfma_f32_16x16x32_bf16 v[12:15], v[72:75], a[84:87], v[12:15]\n" \
  "v_cvt_pk_bf16_f32 v133, v130, v131\n" \
  "v_mfma_f32_16x16x32_bf16 v[16:19], v[72:75], a[116:119], v[16:19]\n" \
  "buffer_load_dwordx4 v[180:183], v149, s[88:91], s92 offen\n" \
  "s_add_u32 s92, s92, 12288\n" \
  "ds_write_b64 v106, v[132:133] offset:0\n" \
  "v_mfma_f32_16x16x32_bf16 v[20:23], v[72:75], a[148:151], v[20:23]\n" \
  "v_mfma_f32_16x16x32_bf16 v[24:27], v[72:75], a[180:183], v[24:27]\n" \
  "v_mfma_f32_16x16x32_bf16 v[28:31], v[72:75], a[212:215], v[28:31]\n" \
  "ds_read2_b32 v[72:73], v99 offset0:64 offset1:66\n" \
  "s_waitcnt lgkmcnt(15)\n" \
  "v_mfma_f32_16x16x32_bf16 v[4:7], v[76:79], a[16:19], v[4:7]\n" \
  "ds_read2_b32 v[74:75], v99 offset0:68 offset1:70\n" \
  "v_mfma_f32_16x16x32_bf16 v[8:11], v[76:79], a[48:51], v[8:11]\n" \
  "v_mfma_f32_16x16x32_bf16 v[12:15], v[76:79], a[80:83], v[12:15]\n" \
  "v_mfma_f32_16x16x32_bf16 v[16:19], v[76:79], a[112:115], v[16:19]\n" \
  "v_mfma_f32_16x16x32_bf16 v[20:23], v[76:79], a[144:147], v[20:23]\n" \
  "v_mfma_f32_16x16x32_bf16 v[24:27], v[76:79], a[176:179], v[24:27]\n" \
  "v_mfma_f32_16x16x32_bf16 v[28:31], v[76:79], a[208:211], v[28:31]\n" \
  "ds_read2_b32 v[76:77], v99 offset0:96 offset1:98\n" \
  "s_waitcnt lgkmcnt(15)\n" \
  "v_mfma_f32_16x16x32_bf16 v[4:7], v[80:83], a[12:15], v[4:7]\n" \
  "ds_read2_b32 v[78:79], v99 offset0:100 offset1:102\n" \
  "v_mfma_f32_16x16x32_bf16 v[8:11], v[80:83], a[44:47], v[8:11]\n" \
  "v_mfma_f32_16x16x32_bf16 v[12:15], v[80:83], a[76:79], v[12:15]\n" \
  "v_mfma_f32_16x16x32_bf16 v[16:19], v[80:83], a[108:111], v[16:19]\n" \
  "v_mfma_f32_16x16x32_bf16 v[20:23], v[80:83], a[140:143], v[20:23]\n" \
  "v_mfma_f32_16x16x32_bf16 v[24:27], v[80:83], a[172:175], v[24:27]\n" \
  "buffer_load_dwordx4 v[184:187], v149, s[88:91], s92 offen\n" \
  "s_add_u32 s92, s92, 12288\n" \
  "v_mfma_f32_16x16x32_bf16 v[28:31], v[80:83], a[204:207], v[28:31]\n" \
  "ds_read2_b32 v[80:81], v99 offset0:128 offset1:130\n" \
  "s_waitcnt lgkmcnt(15)\n" \
  "v_mfma_f32_16x16x32_bf16 v[4:7], v[84:87], a[8:11], v[4:7]\n" \
  "ds_read2_b32 v[82:83], v99 offset0:132 offset1:134\n" \
  "v_mfma_f32_16x16x32_bf16 v[8:11], v[84:87], a[40:43], v[8:11]\n" \
  "v_mfma_f32_16x16x32_bf16 v[12:15], v[84:87], a[72:75], v[12:15]\n" \
  "v_mfma_f32_16x16x32_bf16 v[16:19], v[84:87], a[104:107], v[16:19]\n" \
  "v_mfma_f32_16x16x32_bf16 v[20:23], v[84:87], a[136:139], v[20:23]\n" \
  "v_mfma_f32_16x16x32_bf16 v[24:27], v[84:87], a[168:171], v[24:27]\n" \
  "v_mfma_f32_16x16x32_bf16 v[28:31], v[84:87], a[200:203], v[28:31]\n" \
  "ds_read2_b32 v[84:85], v99 offset0:160 offset1:162\n" \
  "s_waitcnt lgkmcnt(15)\n" \
  "v_mfma_f32_16x16x32_bf16 v[4:7], v[88:91], a[4:7], v[4:7]\n" \
  "ds_read2_b32 v[86:87], v99 offset0:164 offset1:166\n" \
  "v_mfma_f32_16x16x32_bf16 v[8:11], v[88:91], a[36:39], v[8:11]\n" \
  "v_mfma_f32_16x16x32_bf16 v[12:15], v[88:91], a[68:71], v[12:15]\n" \
  "v_mfma_f32_16x16x32_bf16 v[16:19], v[88:91], a[100:103], v[16:19]\n" \
  "v_mfma_f32_16x16x32_bf16 v[20:23], v[88:91], a[132:135], v[20:23]\n" \
  "v_mfma_f32_16x16x32_bf16 v[24:27], v[88:91], a[164:167], v[24:27]\n" \
  "v_mfma_f32_16x16x32_bf16 v[28:31], v[88:91], a[196:199], v[28:31]\n" \
  "ds_read2_b32 v[88:89], v99 offset0:192 offset1:194\n" \
  "s_waitcnt lgkmcnt(15)\n" \
  "v_mfma_f32_16x16x32_bf16 v[4:7], v[92:95], a[0:3], v[4:7]\n" \
  "buffer_load_dwordx4 v[188:191], v149, s[88:91], s92 offen\n" \
  "s_add_u32 s92, s92, 12288\n" \
  "ds_read2_b32 v[90:91], v99 offset0:196 offset1:198\n" \
  "v_mfma_f32_16x16x32_bf16 v[8:11], v[92:95], a[32:35], v[8:11]\n" \
  "v_mfma_f32_16x16x32_bf16 v[12:15], v[92:95], a[64:67], v[12:15]\n" \
  "v_mfma_f32_16x16x32_bf16 v[16:19], v[92:95], a[96:99], v[16:19]\n" \
  "v_mfma_f32_16x16x32_bf16 v[20:23], v[92:95], a[128:131], v[20:23]\n" \
  "v_mfma_f32_16x16x32_bf16 v[24:27], v[92:95], a[160:163], v[24:27]\n" \
  "v_mfma_f32_16x16x32_bf16 v[28:31], v[92:95], a[192:195], v[28:31]\n" \
  "ds_read2_b32 v[92:93], v99 offset0:224 offset1:226\n" \
  "s_waitcnt lgkmcnt(15)\n" \
  "v_mfma_f32_16x16x32_bf16 v[8:11], v[32:35], a[28:31], v[8:11]\n" \
  "ds_read2_b32 v[94:95], v99 offset0:228 offset1:230\n" \
  "v_mfma_f32_16x16x32_bf16 v[12:15], v[32:35], a[60:63], v[12:15]\n" \
  "v_mfma_f32_16x16x32_bf16 v[16:19], v[32:35], a[92:95], v[16:19]\n" \
  "ds_read_b64 v[114:115], v105 offset:512\n" \
  "v_mfma_f32_16x16x32_bf16 v[20:23], v[32:35], a[124:127], v[20:23]\n" \
  "v_mfma_f32_16x16x32_bf16 v[24:27], v[32:35], a[156:159], v[24:27]\n" \
  "v_mfma_f32_16x16x32_bf16 v[28:31], v[32:35], a[188:191], v[28:31]\n" \
  "ds_read2_b32 v[32:33], v100 offset0:0 offset1:2\n" \
  "s_waitcnt lgkmcnt(15)\n" \
  "v_mfma_f32_16x16x32_bf16 v[8:11], v[36:39], a[24:27], v[8:11]\n" \
  "ds_read2_b32 v[34:35], v100 offset0:4 offset1:6\n" \
  "v_mfma_f32_16x16x32_bf16 v[12:15], v[36:39], a[56:59], v[12:15]\n" \
  "v_mfma_f32_16x16x32_bf16 v[16:19], v[36:39], a[88:91], v[16:19]\n" \
  "s_waitcnt lgkmcnt(2)\n" \
  "v_lshlrev_b32 v139, 16, v114\n" \
  "v_mfma_f32_16x16x32_bf16 v[20:23], v[36:39], a[120:123], v[20:23]\n" \
  "buffer_load_dwordx4 v[192:195], v149, s[88:91], s92 offen\n" \
  "s_add_u32 s92, s92, 12288\n" \
  "v_and_b32 v138, 0xffff0000, v114\n" \
  "v_mfma_f32_16x16x32_bf16 v[24:27], v[36:39], a[152:155], v[24:27]\n" \
  "v_lshlrev_b32 v137, 16, v115\n" \
  "v_mfma_f32_16x16x32_bf16 v[28:31], v[36:39], a[184:187], v[28:31]\n" \
  "ds_read2_b32 v[36:37], v100 offset0:32 offset1:34\n" \
  "v_and_b32 v136, 0xffff0000, v115\n" \
  "s_waitcnt lgkmcnt(15)\n" \
  "v_mfma_f32_16x16x32_bf16 v[8:11], v[40:43], a[20:23], v[8:11]\n" \
  "ds_read2_b32 v[38:39], v100 offset0:36 offset1:38\n" \
  "v_fma_f32 v136, v136, s46, v4\n" \
  "v_mfma_f32_16x16x32_bf16 v[12:15], v[40:43], a[52:55], v[12:15]\n" \
  "v_fma_f32 v137, v137, s46, v5\n" \
  "v_mfma_f32_16x16x32_bf16 v[16:19], v[40:43], a[84:87], v[16:19]\n" \
  "v_fma_f32 v138, v138, s46, v6\n" \
  "v_mfma_f32_16x16x32_bf16 v[20:23], v[40:43], a[116:119], v[20:23]\n" \
  "v_fma_f32 v139, v139, s46, v7\n" \
  "v_mfma_f32_16x16x32_bf16 v[24:27], v[40:43], a[148:151], v[24:27]\n" \
  "v_cvt_pk_bf16_f32 v140, v136, v137\n" \
  "v_mfma_f32_16x16x32_bf16 v[28:31], v[40:43], a[180:183], v[28:31]\n" \
  "ds_read2_b32 v[40:41], v100 offset0:64 offset1:66\n" \
  "v_cvt_pk_bf16_f32 v141, v138, v139\n" \
  "s_waitcnt lgkmcnt(15)\n" \
  "v_mfma_f32_16x16x32_bf16 v[8:11], v[44:47], a[16:19], v[8:11]\n" \
  "ds_read2_b32 v[42:43], v100 offset0:68 offset1:70\n" \
  "ds_write_b64 v106, v[140:141] offset:640\n" \
  "v_mfma_f32_16x16x32_bf16 v[12:15], v[44:47], a[48:51], v[12:15]\n" \
  "v_mfma_f32_16x16x32_bf16 v[16:19], v[44:47], a[80:83], v[16:19]\n" \
  "v_mfma_f32_16x16x32_bf16 v[20:23], v[44:47], a[112:115], v[20:23]\n" \
  "v_mfma_f32_16x16x32_bf16 v[24:27], v[44:47], a[144:147], v[24:27]\n" \
  "v_mfma_f32_16x16x32_bf16 v[28:31], v[44:47], a[176:179], v[28:31]\n" \
  "ds_read2_b32 v[44:45], v100 offset0:96 offset1:98\n" \
  "s_waitcnt lgkmcnt(15)\n" \
  "v_mfma_f32_16x16x32_bf16 v[8:11], v[48:51], a[12:15], v[8:11]\n" \
  "ds_read2_b32 v[46:47], v100 offset0:100 offset1:102\n" \
  "v_mfma_f32_16x16x32_bf16 v[12:15], v[48:51], a[44:47], v[12:15]\n" \
  "buffer_load_dwordx4 v[196:199], v149, s[88:91], s92 offen\n" \
  "s_add_u32 s92, s92, 12288\n" \
  "v_mfma_f32_16x16x32_bf16 v[16:19], v[48:51], a[76:79], v[16:19]\n" \
  "v_mfma_f32_16x16x32_bf16 v[20:23], v[48:51], a[108:111], v[20:23]\n" \
  "v_mfma_f32_16x16x32_bf16 v[24:27], v[48:51], a[140:143], v[24:27]\n" \
  "v_mfma_f32_16x16x32_bf16 v[28:31], v[48:51], a[172:175], v[28:31]\n" \
  "ds_read2_b32 v[48:49], v100 offset0:128 offset1:130\n" \
  "s_waitcnt lgkmcnt(15)\n" \
  "v_mfma_f32_16x16x32_bf16 v[8:11], v[52:55], a[8:11], v[8:11]\n" \
  "ds_read2_b32 v[50:51], v100 offset0:132 offset1:134\n" \
  "v_mfma_f32_16x16x32_bf16 v[12:15], v[52:55], a[40:43], v[12:15]\n" \
  "v_mfma_f32_16x16x32_bf16 v[16:19], v[52:55], a[72:75], v[16:19]\n" \
  "v_mfma_f32_16x16x32_bf16 v[20:23], v[52:55], a[104:107], v[20:23]\n" \
  "v_mfma_f32_16x16x32_bf16 v[24:27], v[52:55], a[136:139], v[24:27]\n" \
  "v_mfma_f32_16x16x32_bf16 v[28:31], v[52:55], a[168:171], v[28:31]\n" \
  "ds_read2_b32 v[52:53], v100 offset0:160 offset1:162\n" \
  "s_waitcnt lgkmcnt(15)\n" \
  "v_mfma_f32_16x16x32_bf16 v[8:11], v[56:59], a[4:7], v[8:11]\n" \
  "ds_read2_b32 v[54:55], v100 offset0:164 offset1:166\n" \
  "v_mfma_f32_16x16x32_bf16 v[12:15], v[56:59], a[36:39], v[12:15]\n" \
  "v_mfma_f32_16x16x32_bf16 v[16:19], v[56:59], a[68:71], v[16:19]\n" \
  "v_mfma_f32_16x16x32_bf16 v[20:23], v[56:59], a[100:103], v[20:23]\n" \
  "v_mfma_f32_16x16x32_bf16 v[24:27], v[56:59], a[132:135], v[24:27]\n" \
  "v_mfma_f32_16x16x32_bf16 v[28:31], v[56:59], a[164:167], v[28:31]\n" \
  "buffer_load_dwordx4 v[200:203], v149, s[88:91], s92 offen\n" \
  "s_add_u32 s92, s92, 12288\n" \
  "ds_read2_b32 v[56:57], v100 offset0:192 offset1:194\n" \
  "s_waitcnt lgkmcnt(15)\n" \
  "v_mfma_f32_16x16x32_bf16 v[8:11], v[60:63], a[0:3], v[8:11]\n" \
  "ds_read2_b32 v[58:59], v100 offset0:196 offset1:198\n" \
  "v_mfma_f32_16x16x32_bf16 v[12:15], v[60:63], a[32:35], v[12:15]\n" \
  "v_mfma_f32_16x16x32_bf16 v[16:19], v[60:63], a[64:67], v[16:19]\n" \
  "v_mfma_f32_16x16x32_bf16 v[20:23], v[60:63], a[96:99], v[20:23]\n" \
  "v_mfma_f32_16x16x32_bf16 v[24:27], v[60:63], a[128:131], v[24:27]\n" \
  "v_mfma_f32_16x16x32_bf16 v[28:31], v[60:63], a[160:163], v[28:31]\n" \
  "ds_read2_b32 v[60:61], v100 offset0:224 offset1:226\n" \
  "s_waitcnt lgkmcnt(15)\n" \
  "v_mfma_f32_16x16x32_bf16 v[12:15], v[64:67], a[28:31], v[12:15]\n" \
  "ds_read2_b32 v[62:63], v100 offset0:228 offset1:230\n" \
  "v_mfma_f32_16x16x32_bf16 v[16:19], v[64:67], a[60:63], v[16:19]\n" \
  "v_mfma_f32_16x16x32_bf16 v[20:23], v[64:67], a[92:95], v[20:23]\n" \
  "v_mfma_f32_16x16x32_bf16 v[24:27], v[64:67], a[124:127], v[24:27]\n" \
  "ds_read_b64 v[116:117], v105 offset:1024\n" \
  "v_mfma_f32_16x16x32_bf16 v[28:31], v[64:67], a[156:159], v[28:31]\n" \
  "ds_read2_b32 v[64:65], v101 offset0:0 offset1:2\n" \
  "s_waitcnt lgkmcnt(15)\n" \
  "v_mfma_f32_16x16x32_bf16 v[12:15], v[68:71], a[24:27], v[12:15]\n" \
  "ds_read2_b32 v[66:67], v101 offset0:4 offset1:6\n" \
  "v_mfma_f32_16x16x32_bf16 v[16:19], v[68:71], a[56:59], v[16:19]\n" \
  "v_mfma_f32_16x16x32_bf16 v[20:23], v[68:71], a[88:91], v[20:23]\n" \
  "v_mfma_f32_16x16x32_bf16 v[24:27], v[68:71], a[120:123], v[24:27]\n" \
  "v_mfma_f32_16x16x32_bf16 v[28:31], v[68:71], a[152:155], v[28:31]\n" \
  "buffer_load_dwordx4 v[204:207], v149, s[88:91], s92 offen\n" \
  "s_add_u32 s92, s92, 12288\n" \
  "ds_read2_b32 v[68:69], v101 offset0:32 offset1:34\n" \
  "s_waitcnt lgkmcnt(3)\n" \
  "v_lshlrev_b32 v131, 16, v116\n" \
  "s_waitcnt lgkmcnt(15)\n" \
  "v_mfma_f32_16x16x32_bf16 v[12:15], v[72:75], a[20:23], v[12:15]\n" \
  "ds_read2_b32 v[70:71], v101 offset0:36 offset1:38\n" \
  "v_and_b32 v130, 0xffff0000, v116\n" \
  "v_mfma_f32_16x16x32_bf16 v[16:19], v[72:75], a[52:55], v[16:19]\n" \
  "v_lshlrev_b32 v129, 16, v117\n" \
  "v_mfma_f32_16x16x32_bf16 v[20:23], v[72:75], a[84:87], v[20:23]\n" \
  "v_and_b32 v128, 0xffff0000, v117\n" \
  "v_mfma_f32_16x16x32_bf16 v[24:27], v[72:75], a[116:119], v[24:27]\n" \
  "v_fma_f32 v128, v128, s46, v8\n" \
  "v_mfma_f32_16x16x32_bf16 v[28:31], v[72:75], a[148:151], v[28:31]\n" \
  "ds_read2_b32 v[72:73], v101 offset0:64 offset1:66\n" \
  "v_fma_f32 v129, v129, s46, v9\n" \
  "s_waitcnt lgkmcnt(15)\n" \
  "v_mfma_f32_16x16x32_bf16 v[12:15], v[76:79], a[16:19], v[12:15]\n" \
  "ds_read2_b32 v[74:75], v101 offset0:68 offset1:70\n" \
  "v_fma_f32 v130, v130, s46, v10\n" \
  "v_mfma_f32_16x16x32_bf16 v[16:19], v[76:79], a[48:51], v[16:19]\n" \
  "v_fma_f32 v131, v131, s46, v11\n" \
  "v_mfma_f32_16x16x32_bf16 v[20:23], v[76:79], a[80:83], v[20:23]\n" \
  "v_cvt_pk_bf16_f32 v132, v128, v129\n" \
  "v_mfma_f32_16x16x32_bf16 v[24:27], v[76:79], a[112:115], v[24:27]\n" \
  "v_cvt_pk_bf16_f32 v133, v130, v131\n" \
  "v_mfma_f32_16x16x32_bf16 v[28:31], v[76:79], a[144:147], v[28:31]\n" \
  "ds_read2_b32 v[76:77], v101 offset0:96 offset1:98\n" \
  "ds_write_b64 v106, v[132:133] offset:1280\n" \
  "s_waitcnt lgkmcnt(15)\n" \
  "v_mfma_f32_16x16x32_bf16 v[12:15], v[80:83], a[12:15], v[12:15]\n" \
  "ds_read2_b32 v[78:79], v101 offset0:100 offset1:102\n" \
  "v_mfma_f32_16x16x32_bf16 v[16:19], v[80:83], a[44:47], v[16:19]\n" \
  "v_mfma_f32_16x16x32_bf16 v[20:23], v[80:83], a[76:79], v[20:23]\n" \
  "v_mfma_f32_16x16x32_bf16 v[24:27], v[80:83], a[108:111], v[24:27]\n" \
  "v_mfma_f32_16x16x32_bf16 v[28:31], v[80:83], a[140:143], v[28:31]\n" \
  "ds_read2_b32 v[80:81], v101 offset0:128 offset1:130\n" \
  "s_waitcnt lgkmcnt(15)\n" \
  "v_mfma_f32_16x16x32_bf16 v[12:15], v[84:87], a[8:11], v[12:15]\n" \
  "buffer_load_dwordx4 v[208:211], v149, s[88:91], s92 offen\n" \
  "s_add_u32 s92, s92, 12288\n" \
  "ds_read2_b32 v[82:83], v101 offset0:132 offset1:134\n" \
  "v_mfma_f32_16x16x32_bf16 v[16:19], v[84:87], a[40:43], v[16:19]\n" \
  "v_mfma_f32_16x16x32_bf16 v[20:23], v[84:87], a[72:75], v[20:23]\n" \
  "v_mfma_f32_16x16x32_bf16 v[24:27], v[84:87], a[104:107], v[24:27]\n" \
  "v_mfma_f32_16x16x32_bf16 v[28:31], v[84:87], a[136:139], v[28:31]\n" \
  "ds_read2_b32 v[84:85], v101 offset0:160 offset1:162\n" \
  "s_waitcnt lgkmcnt(15)\n" \
  "v_mfma_f32_16x16x32_bf16 v[12:15], v[88:91], a[4:7], v[12:15]\n" \
  "ds_read2_b32 v[86:87], v101 offset0:164 offset1:166\n" \
  "v_mfma_f32_16x16x32_bf16 v[16:19], v[88:91], a[36:39], v[16:19]\n" \
  "v_mfma_f32_16x16x32_bf16 v[20:23], v[88:91], a[68:71], v[20:23]\n" \
  "v_mfma_f32_16x16x32_bf16 v[24:27], v[88:91], a[100:103], v[24:27]\n" \
  "v_mfma_f32_16x16x32_bf16 v[28:31], v[88:91], a[132:135], v[28:31]\n" \
  "ds_read2_b32 v[88:89], v101 offset0:192 offset1:194\n" \
  "s_waitcnt lgkmcnt(15)\n" \
  "v_mfma_f32_16x16x32_bf16 v[12:15], v[92:95], a[0:3], v[12:15]\n" \
  "ds_read2_b32 v[90:91], v101 offset0:196 offset1:198\n" \
  "v_mfma_f32_16x16x32_bf16 v[16:19], v[92:95], a[32:35], v[16:19]\n" \
  "v_mfma_f32_16x16x32_bf16 v[20:23], v[92:95], a[64:67], v[20:23]\n" \
  "v_mfma_f32_16x16x32_bf16 v[24:27], v[92:95], a[96:99], v[24:27]\n" \
  "v_mfma_f32_16x16x32_bf16 v[28:31], v[92:95], a[128:131], v[28:31]\n" \
  "ds_read2_b32 v[92:93], v101 offset0:224 offset1:226\n" \
  "s_waitcnt lgkmcnt(15)\n" \
  "v_mfma_f32_16x16x32_bf16 v[16:19], v[32:35], a[28:31], v[16:19]\n" \
  "ds_read2_b32 v[94:95], v101 offset0:228 offset1:230\n" \
  "v_mfma_f32_16x16x32_bf16 v[20:23], v[32:35], a[60:63], v[20:23]\n" \
  "buffer_load_dwordx4 v[212:215], v149, s[88:91], s92 offen\n" \
  "s_add_u32 s92, s92, 12288\n" \
  "v_mfma_f32_16x16x32_bf16 v[24:27], v[32:35], a[92:95], v[24:27]\n" \
  "v_mfma_f32_16x16x32_bf16 v[28:31], v[32:35], a[124:127], v[28:31]\n" \
  "ds_read2_b32 v[32:33], v102 offset0:0 offset1:2\n" \
  "s_waitcnt lgkmcnt(15)\n" \
  "v_mfma_f32_16x16x32_bf16 v[16:19], v[36:39], a[24:27], v[16:19]\n" \
  "ds_read2_b32 v[34:35], v102 offset0:4 offset1:6\n" \
  "ds_read_b64 v[118:119], v105 offset:1536\n" \
  "v_mfma_f32_16x16x32_bf16 v[20:23], v[36:39], a[56:59], v[20:23]\n" \
  "v_mfma_f32_16x16x32_bf16 v[24:27], v[36:39], a[88:91], v[24:27]\n" \
  "v_mfma_f32_16x16x32_bf16 v[28:31], v[36:39], a[120:123], v[28:31]\n" \
  "ds_read2_b32 v[36:37], v102 offset0:32 offset1:34\n" \
  "s_waitcnt lgkmcnt(15)\n" \
  "v_mfma_f32_16x16x32_bf16 v[16:19], v[40:43], a[20:23], v[16:19]\n" \
  "ds_read2_b32 v[38:39], v102 offset0:36 offset1:38\n" \
  "v_mfma_f32_16x16x32_bf16 v[20:23], v[40:43], a[52:55], v[20:23]\n" \
  "v_mfma_f32_16x16x32_bf16 v[24:27], v[40:43], a[84:87], v[24:27]\n" \
  "s_waitcnt lgkmcnt(2)\n" \
  "v_lshlrev_b32 v139, 16, v118\n" \
  "v_mfma_f32_16x16x32_bf16 v[28:31], v[40:43], a[116:119], v[28:31]\n" \
  "ds_read2_b32 v[40:41], v102 offset0:64 offset1:66\n" \
  "v_and_b32 v138, 0xffff0000, v118\n" \
  "s_waitcnt lgkmcnt(15)\n" \
  "v_mfma_f32_16x16x32_bf16 v[16:19], v[44:47], a[16:19], v[16:19]\n" \
  "ds_read2_b32 v[42:43], v102 offset0:68 offset1:70\n" \
  "v_lshlrev_b32 v137, 16, v119\n" \
  "v_mfma_f32_16x16x32_bf16 v[20:23], v[44:47], a[48:51], v[20:23]\n" \
  "v_and_b32 v136, 0xffff0000, v119\n" \
  "v_mfma_f32_16x16x32_bf16 v[24:27], v[44:47], a[80:83], v[24:27]\n" \
  "v_fma_f32 v136, v136, s46, v12\n" \
  "v_mfma_f32_16x16x32_bf16 v[28:31], v[44:47], a[112:115], v[28:31]\n" \
  "ds_read2_b32 v[44:45], v102 offset0:96 offset1:98\n" \
  "v_fma_f32 v137, v137, s46, v13\n" \
  "s_waitcnt lgkmcnt(15)\n" \
  "v_mfma_f32_16x16x32_bf16 v[16:19], v[48:51], a[12:15], v[16:19]\n" \
  "ds_read2_b32 v[46:47], v102 offset0:100 offset1:102\n" \
  "v_fma_f32 v138, v138, s46, v14\n" \
  "v_mfma_f32_16x16x32_bf16 v[20:23], v[48:51], a[44:47], v[20:23]\n" \
  "buffer_load_dwordx4 v[216:219], v149, s[88:91], s92 offen\n" \
  "s_add_u32 s92, s92, 12288\n" \
  "v_fma_f32 v139, v139, s46, v15\n" \
  "v_mfma_f32_16x16x32_bf16 v[24:27], v[48:51], a[76:79], v[24:27]\n" \
  "v_cvt_pk_bf16_f32 v140, v136, v137\n" \
  "v_mfma_f32_16x16x32_bf16 v[28:31], v[48:51], a[108:111], v[28:31]\n" \
  "ds_read2_b32 v[48:49], v102 offset0:128 offset1:130\n" \
  "v_cvt_pk_bf16_f32 v141, v138, v139\n" \
  "s_waitcnt lgkmcnt(15)\n" \
  "v_mfma_f32_16x16x32_bf16 v[16:19], v[52:55], a[8:11], v[16:19]\n" \
  "ds_read2_b32 v[50:51], v102 offset0:132 offset1:134\n" \
  "ds_write_b64 v106, v[140:141] offset:1920\n" \
  "v_mfma_f32_16x16x32_bf16 v[20:23], v[52:55], a[40:43], v[20:23]\n" \
  "v_mfma_f32_16x16x32_bf16 v[24:27], v[52:55], a[72:75], v[24:27]\n" \
  "v_mfma_f32_16x16x32_bf16 v[28:31], v[52:55], a[104:107], v[28:31]\n" \
  "ds_read2_b32 v[52:53], v102 offset0:160 offset1:162\n" \
  "s_waitcnt lgkmcnt(15)\n" \
  "v_mfma_f32_16x16x32_bf16 v[16:19], v[56:59], a[4:7], v[16:19]\n" \
  "ds_read2_b32 v[54:55], v102 offset0:164 offset1:166\n" \
  "v_mfma_f32_16x16x32_bf16 v[20:23], v[56:59], a[36:39], v[20:23]\n" \
  "v_mfma_f32_16x16x32_bf16 v[24:27], v[56:59], a[68:71], v[24:27]\n" \
  "v_mfma_f32_16x16x32_bf16 v[28:31], v[56:59], a[100:103], v[28:31]\n" \
  "ds_read2_b32 v[56:57], v102 offset0:192 offset1:194\n" \
  "s_waitcnt lgkmcnt(15)\n" \
  "v_mfma_f32_16x16x32_bf16 v[16:19], v[60:63], a[0:3], v[16:19]\n" \
  "ds_read2_b32 v[58:59], v102 offset0:196 offset1:198\n" \
  "v_mfma_f32_16x16x32_bf16 v[20:23], v[60:63], a[32:35], v[20:23]\n" \
  "v_mfma_f32_16x16x32_bf16 v[24:27], v[60:63], a[64:67], v[24:27]\n" \
  "v_mfma_f32_16x16x32_bf16 v[28:31], v[60:63], a[96:99], v[28:31]\n" \
  "ds_read2_b32 v[60:61], v102 offset0:224 offset1:226\n" \
  "s_waitcnt lgkmcnt(15)\n" \
  "v_mfma_f32_16x16x32_bf16 v[20:23], v[64:67], a[28:31], v[20:23]\n" \
  "ds_read2_b32 v[62:63], v102 offset0:228 offset1:230\n" \
  "v_mfma_f32_16x16x32_bf16 v[24:27], v[64:67], a[60:63], v[24:27]\n" \
  "buffer_load_dwordx4 v[220:223], v149, s[88:91], s92 offen\n" \
  "v_mfma_f32_16x16x32_bf16 v[28:31], v[64:67], a[92:95], v[28:31]\n" \
  "ds_read2_b32 v[64:65], v103 offset0:0 offset1:2\n" \
  "s_waitcnt lgkmcnt(15)\n" \
  "v_mfma_f32_16x16x32_bf16 v[20:23], v[68:71], a[24:27], v[20:23]\n" \
  "ds_read2_b32 v[66:67], v103 offset0:4 offset1:6\n" \
  "v_mfma_f32_16x16x32_bf16 v[24:27], v[68:71], a[56:59], v[24:27]\n" \
  "v_mfma_f32_16x16x32_bf16 v[28:31], v[68:71], a[88:91], v[28:31]\n" \
  "ds_read2_b32 v[68:69], v103 offset0:32 offset1:34\n" \
  "ds_read_b64 v[120:121], v105 offset:2048\n" \
  "s_waitcnt lgkmcnt(15)\n" \
  "v_mfma_f32_16x16x32_bf16 v[20:23], v[72:75], a[20:23], v[20:23]\n" \
  "ds_read2_b32 v[70:71], v103 offset0:36 offset1:38\n" \
  "v_mfma_f32_16x16x32_bf16 v[24:27], v[72:75], a[52:55], v[24:27]\n" \
  "v_mfma_f32_16x16x32_bf16 v[28:31], v[72:75], a[84:87], v[28:31]\n" \
  "ds_read2_b32 v[72:73], v103 offset0:64 offset1:66\n" \
  "s_waitcnt lgkmcnt(15)\n" \
  "v_mfma_f32_16x16x32_bf16 v[20:23], v[76:79], a[16:19], v[20:23]\n" \
  "ds_read2_b32 v[74:75], v103 offset0:68 offset1:70\n" \
  "v_mfma_f32_16x16x32_bf16 v[24:27], v[76:79], a[48:51], v[24:27]\n" \
  "v_mfma_f32_16x16x32_bf16 v[28:31], v[76:79], a[80:83], v[28:31]\n" \
  "ds_read2_b32 v[76:77], v103 offset0:96 offset1:98\n" \
  "s_waitcnt lgkmcnt(4)\n" \
  "v_lshlrev_b32 v131, 16, v120\n" \
  "s_waitcnt lgkmcnt(15)\n" \
  "v_mfma_f32_16x16x32_bf16 v[20:23], v[80:83], a[12:15], v[20:23]\n" \
  "ds_read2_b32 v[78:79], v103 offset0:100 offset1:102\n" \
  "v_and_b32 v130, 0xffff0000, v120\n" \
  "v_mfma_f32_16x16x32_bf16 v[24:27], v[80:83], a[44:47], v[24:27]\n" \
  "v_lshlrev_b32 v129, 16, v121\n" \
  "v_mfma_f32_16x16x32_bf16 v[28:31], v[80:83], a[76:79], v[28:31]\n" \
  "ds_read2_b32 v[80:81], v103 offset0:128 offset1:130\n" \
  "v_and_b32 v128, 0xffff0000, v121\n" \
  "s_waitcnt lgkmcnt(15)\n" \
  "v_mfma_f32_16x16x32_bf16 v[20:23], v[84:87], a[8:11], v[20:23]\n" \
  "ds_read2_b32 v[82:83], v103 offset0:132 offset1:134\n" \
  "v_fma_f32 v128, v128, s46, v16\n" \
  "v_mfma_f32_16x16x32_bf16 v[24:27], v[84:87], a[40:43], v[24:27]\n" \
  "v_fma_f32 v129, v129, s46, v17\n" \
  "v_mfma_f32_16x16x32_bf16 v[28:31], v[84:87], a[72:75], v[28:31]\n" \
  "ds_read2_b32 v[84:85], v103 offset0:160 offset1:162\n" \
  "v_fma_f32 v130, v130, s46, v18\n" \
  "s_waitcnt lgkmcnt(15)\n" \
  "v_mfma_f32_16x16x32_bf16 v[20:23], v[88:91], a[4:7], v[20:23]\n" \
  "ds_read2_b32 v[86:87], v103 offset0:164 offset1:166\n" \
  "v_fma_f32 v131, v131, s46, v19\n" \
  "v_mfma_f32_16x16x32_bf16 v[24:27], v[88:91], a[36:39], v[24:27]\n" \
  "v_cvt_pk_bf16_f32 v132, v128, v129\n" \
  "v_mfma_f32_16x16x32_bf16 v[28:31], v[88:91], a[68:71], v[28:31]\n" \
  "ds_read2_b32 v[88:89], v103 offset0:192 offset1:194\n" \
  "v_cvt_pk_bf16_f32 v133, v130, v131\n" \
  "s_waitcnt lgkmcnt(15)\n" \
  "v_mfma_f32_16x16x32_bf16 v[20:23], v[92:95], a[0:3], v[20:23]\n" \
  "ds_read2_b32 v[90:91], v103 offset0:196 offset1:198\n" \
  "ds_write_b64 v106, v[132:133] offset:2560\n" \
  "v_mfma_f32_16x16x32_bf16 v[24:27], v[92:95], a[32:35], v[24:27]\n" \
  "v_mfma_f32_16x16x32_bf16 v[28:31], v[92:95], a[64:67], v[28:31]\n" \
  "ds_read2_b32 v[92:93], v103 offset0:224 offset1:226\n" \
  "s_waitcnt lgkmcnt(15)\n" \
  "v_mfma_f32_16x16x32_bf16 v[24:27], v[32:35], a[28:31], v[24:27]\n" \
  "ds_read2_b32 v[94:95], v103 offset0:228 offset1:230\n" \
  "v_mfma_f32_16x16x32_bf16 v[28:31], v[32:35], a[60:63], v[28:31]\n" \
  "s_waitcnt lgkmcnt(15)\n" \
  "v_mfma_f32_16x16x32_bf16 v[24:27], v[36:39], a[24:27], v[24:27]\n" \
  "v_mfma_f32_16x16x32_bf16 v[28:31], v[36:39], a[56:59], v[28:31]\n" \
  "s_waitcnt lgkmcnt(15)\n" \
  "v_mfma_f32_16x16x32_bf16 v[24:27], v[40:43], a[20:23], v[24:27]\n" \
  "v_mfma_f32_16x16x32_bf16 v[28:31], v[40:43], a[52:55], v[28:31]\n" \
  "s_waitcnt lgkmcnt(15)\n" \
  "v_mfma_f32_16x16x32_bf16 v[24:27], v[44:47], a[16:19], v[24:27]\n" \
  "v_mfma_f32_16x16x32_bf16 v[28:31], v[44:47], a[48:51], v[28:31]\n" \
  "s_waitcnt lgkmcnt(15)\n" \
  "v_mfma_f32_16x16x32_bf16 v[24:27], v[48:51], a[12:15], v[24:27]\n" \
  "v_mfma_f32_16x16x32_bf16 v[28:31], v[48:51], a[44:47], v[28:31]\n" \
  "s_waitcnt lgkmcnt(15)\n" \
  "v_mfma_f32_16x16x32_bf16 v[24:27], v[52:55], a[8:11], v[24:27]\n" \
  "v_mfma_f32_16x16x32_bf16 v[28:31], v[52:55], a[40:43], v[28:31]\n" \
  "s_waitcnt lgkmcnt(15)\n" \
  "v_mfma_f32_16x16x32_bf16 v[24:27], v[56:59], a[4:7], v[24:27]\n" \
  "v_mfma_f32_16x16x32_bf16 v[28:31], v[56:59], a[36:39], v[28:31]\n" \
  "s_waitcnt lgkmcnt(15)\n" \
  "v_mfma_f32_16x16x32_bf16 v[24:27], v[60:63], a[0:3], v[24:27]\n" \
  "v_mfma_f32_16x16x32_bf16 v[28:31], v[60:63], a[32:35], v[28:31]\n" \
  "s_waitcnt lgkmcnt(15)\n" \
  "v_mfma_f32_16x16x32_bf16 v[28:31], v[64:67], a[28:31], v[28:31]\n" \
  "s_waitcnt lgkmcnt(13)\n" \
  "v_mfma_f32_16x16x32_bf16 v[28:31], v[68:71], a[24:27], v[28:31]\n" \
  "s_waitcnt lgkmcnt(11)\n" \
  "v_mfma_f32_16x16x32_bf16 v[28:31], v[72:75], a[20:23], v[28:31]\n" \
  "s_waitcnt lgkmcnt(9)\n" \
  "v_mfma_f32_16x16x32_bf16 v[28:31], v[76:79], a[16:19], v[28:31]\n" \
  "s_waitcnt lgkmcnt(7)\n" \
  "v_mfma_f32_16x16x32_bf16 v[28:31], v[80:83], a[12:15], v[28:31]\n" \
  "s_waitcnt lgkmcnt(5)\n" \
  "v_mfma_f32_16x16x32_bf16 v[28:31], v[84:87], a[8:11], v[28:31]\n" \
  "s_waitcnt lgkmcnt(3)\n" \
  "v_mfma_f32_16x16x32_bf16 v[28:31], v[88:91], a[4:7], v[28:31]\n" \
  "s_waitcnt lgkmcnt(0)\n" \
  "v_mfma_f32_16x16x32_bf16 v[28:31], v[92:95], a[0:3], v[28:31]\n" \
  "ds_read_b64 v[122:123], v105 offset:2560\n" \
  "ds_read_b64 v[124:125], v105 offset:3072\n" \
  "ds_read_b64 v[126:127], v105 offset:3584\n" \
  "s_nop 7\n" \
  "s_nop 3\n" \
  "s_waitcnt lgkmcnt(2)\n" \
  "v_lshlrev_b32 v139, 16, v122\n" \
  "v_and_b32 v138, 0xffff0000, v122\n" \
  "v_lshlrev_b32 v137, 16, v123\n" \
  "v_and_b32 v136, 0xffff0000, v123\n" \
  "v_fma_f32 v136, v136, s46, v20\n" \
  "v_fma_f32 v137, v137, s46, v21\n" \
  "v_fma_f32 v138, v138, s46, v22\n" \
  "v_fma_f32 v139, v139, s46, v23\n" \
  "v_cvt_pk_bf16_f32 v140, v136, v137\n" \
  "v_cvt_pk_bf16_f32 v141, v138, v139\n" \
  "ds_write_b64 v106, v[140:141] offset:3200\n" \
  "s_waitcnt lgkmcnt(2)\n" \
  "v_lshlrev_b32 v131, 16, v124\n" \
  "v_and_b32 v130, 0xffff0000, v124\n" \
  "v_lshlrev_b32 v129, 16, v125\n" \
  "v_and_b32 v128, 0xffff0000, v125\n" \
  "v_fma_f32 v128, v128, s46, v24\n" \
  "v_fma_f32 v129, v129, s46, v25\n" \
  "v_fma_f32 v130, v130, s46, v26\n" \
  "v_fma_f32 v131, v131, s46, v27\n" \
  "v_cvt_pk_bf16_f32 v132, v128, v129\n" \
  "v_cvt_pk_bf16_f32 v133, v130, v131\n" \
  "ds_write_b64 v106, v[132:133] offset:3840\n" \
  "s_waitcnt lgkmcnt(2)\n" \
  "v_lshlrev_b32 v139, 16, v126\n" \
  "v_and_b32 v138, 0xffff0000, v126\n" \
  "v_lshlrev_b32 v137, 16, v127\n" \
  "v_and_b32 v136, 0xffff0000, v127\n" \
  "v_fma_f32 v136, v136, s46, v28\n" \
  "v_fma_f32 v137, v137, s46, v29\n" \
  "v_fma_f32 v138, v138, s46, v30\n" \
  "v_fma_f32 v139, v139, s46, v31\n" \
  "v_cvt_pk_bf16_f32 v140, v136, v137\n" \
  "v_cvt_pk_bf16_f32 v141, v138, v139\n" \
  "ds_write_b64 v106, v[140:141] offset:4480\n" \
  "s_waitcnt vmcnt(0) lgkmcnt(0)\n" \
  ""
#define K2Q_ASM \
  "s_mov_b32 s48, 0\n" \
  "s_mov_b32 s49, 0x80000000\n" \
  "v_mov_b32 v32, 0\n" \
  "v_mov_b32 v33, 0\n" \
  "ds_write_b64 v111, v[32:33]\n" \
  "v_readfirstlane_b32 s47, v112\n" \
  "s_lshl_b32 s47, s47, 16\n" \
  "v_mov_b32 v110, s47\n" \
  "v_cndmask_b32 v110, 0, v110, s[48:49]\n" \
  "ds_write_b32 v109, v110 offset:0\n" \
  "v_mov_b32 v114, v113\n" \
  "v_alignbit_b32 v115, v115, v113, 16\n" \
  "v_alignbit_b32 v113, v113, v112, 16\n" \
  "ds_write_b128 v144, v[112:115] offset:256\n" \
  "v_mov_b32 v118, v117\n" \
  "v_alignbit_b32 v119, v119, v117, 16\n" \
  "v_alignbit_b32 v117, v117, v116, 16\n" \
  "ds_write_b128 v144, v[116:119] offset:1280\n" \
  "v_mov_b32 v122, v121\n" \
  "v_alignbit_b32 v123, v123, v121, 16\n" \
  "v_alignbit_b32 v121, v121, v120, 16\n" \
  "ds_write_b128 v144, v[120:123] offset:2304\n" \
  "v_mov_b32 v126, v125\n" \
  "v_alignbit_b32 v127, v127, v125, 16\n" \
  "v_alignbit_b32 v125, v125, v124, 16\n" \
  "ds_write_b128 v144, v[124:127] offset:3328\n" \
  "v_mov_b32 v130, v129\n" \
  "v_alignbit_b32 v131, v131, v129, 16\n" \
  "v_alignbit_b32 v129, v129, v128, 16\n" \
  "ds_write_b128 v144, v[128:131] offset:4352\n" \
  "v_mov_b32 v134, v133\n" \
  "v_alignbit_b32 v135, v135, v133, 16\n" \
  "v_alignbit_b32 v133, v133, v132, 16\n" \
  "ds_write_b128 v144, v[132:135] offset:5376\n" \
  "v_mov_b32 v138, v137\n" \
  "v_alignbit_b32 v139, v139, v137, 16\n" \
  "v_alignbit_b32 v137, v137, v136, 16\n" \
  "ds_write_b128 v144, v[136:139] offset:6400\n" \
  "v_mov_b32 v142, v141\n" \
  "v_alignbit_b32 v143, v143, v141, 16\n" \
  "v_alignbit_b32 v141, v141, v140, 16\n" \
  "ds_write_b128 v144, v[140:143] offset:7424\n" \
  ""
#define PF_CHUNK "3584"
#define PF2_SIZE "0x700000"
#define WEXP 0
#define FL_BASE 16384
#define FL_SSTR 2112
#define FL_PSTR 256
#define FL_EOFF 1040
#define FL_SLEEP 4
#define FL_SLEEP0 0
#define G1_CLOB_V "v0","v1","v2","v3","v4","v5","v6","v7","v8","v9","v10","v11","v12","v13","v14","v15","v16","v17","v18","v19","v20","v21","v22","v23","v24","v25","v26","v27","v28","v29","v30","v31","v32","v33","v34","v35","v36","v37","v38","v39","v40","v41","v42","v43","v44","v45","v46","v47","v48","v49","v50","v51","v52","v53","v54","v55","v56","v57","v58","v59","v60","v61","v62","v63","v64","v65","v66","v67","v68","v69","v70","v71","v72","v73","v74","v75","v76","v77","v78","v79","v80","v81","v82","v83","v84","v85","v86","v87","v88","v89","v90","v91","v92","v93","v94","v95","v96","v97","v98","v99","v100","v101","v102","v103","v104","v105","v106","v107","v108","v109","v110","v111","v112","v113","v114","v115","v116","v117","v118","v119","v120","v121","v122","v123","v124","v125","v126","v127","v128","v129","v130","v131","v132","v133","v134","v135","v136","v137","v138","v139","v140","v141","v142","v143","v144","v145","v146","v147","v148","v149","v150","v151","v152","v153","v154","v155","v156","v157","v158","v159","v160","v161","v162","v163","v164","v165","v166","v167","v168","v169","v170","v171","v172","v173","v174","v175","v176","v177","v178","v179","v180","v181","v182","v183","v184","v185","v186","v187","v188","v189","v190","v191","v192","v193","v194","v195","v196","v197","v198","v199","v200","v201","v202","v203","v204","v205","v206","v207","v208","v209","v210","v211","v212","v213","v214","v215","v216","v217","v218","v219","v220","v221","v222","v223","v232","v233","v234","v235","v236","v237","v238","v239","v247","v248","v249","v250","v251"
#define G1_CLOB_A "a0","a1","a2","a3","a4","a5","a6","a7","a8","a9","a10","a11","a12","a13","a14","a15","a16","a17","a18","a19","a20","a21","a22","a23","a24","a25","a26","a27","a28","a29","a30","a31","a32","a33","a34","a35","a36","a37","a38","a39","a40","a41","a42","a43","a44","a45","a46","a47","a48","a49","a50","a51","a52","a53","a54","a55","a56","a57","a58","a59","a60","a61","a62","a63","a64","a65","a66","a67","a68","a69","a70","a71","a72","a73","a74","a75","a76","a77","a78","a79","a80","a81","a82","a83","a84","a85","a86","a87","a88","a89","a90","a91","a92","a93","a94","a95","a96","a97","a98","a99","a100","a101","a102","a103","a104","a105","a106","a107","a108","a109","a110","a111","a112","a113","a114","a115","a116","a117","a118","a119","a120","a121","a122","a123","a124","a125","a126","a127","a128","a129","a130","a131","a132","a133","a134","a135","a136","a137","a138","a139","a140","a141","a142","a143","a144","a145","a146","a147","a148","a149","a150","a151","a152","a153","a154","a155","a156","a157","a158","a159","a160","a161","a162","a163","a164","a165","a166","a167","a168","a169","a170","a171","a172","a173","a174","a175","a176","a177","a178","a179","a180","a181","a182","a183","a184","a185","a186","a187","a188","a189","a190","a191","a192","a193","a194","a195"
#define G1_CLOB_S "s36","s37","s38","s39","s40","s41","s42","s43","s44","s45","s46","s47","s48","s49","s50","s51","s52","s53","s54","s55","s56","s57","s58","s59","s60","s61","s62","s63","s64","s65","s66","s67","s68","s69","s70","s71","s72","s73","s74","s75","s76","s77","s78","s79","s80","s81","s82","s83","s84","s85","s86","s87","s88","s89"
#define G1_SENT "s_mov_b64 s[84:85], %[xc]\n s_mov_b32 s86, 0x800000\n s_mov_b32 s87, 0x20000\n s_lshl_b32 s88, %[gw], 13\n s_add_u32 s89, s88, 4096\n"
#define G2_CLOB_V "v0","v1","v2","v3","v4","v5","v6","v7","v8","v9","v10","v11","v12","v13","v14","v15","v16","v17","v18","v19","v20","v21","v22","v23","v24","v25","v26","v27","v28","v29","v30","v31","v32","v33","v34","v35","v36","v37","v38","v39","v40","v41","v42","v43","v44","v45","v46","v47","v48","v49","v50","v51","v52","v53","v54","v55","v56","v57","v58","v59","v60","v61","v62","v63","v72","v73","v74","v75","v76","v77","v78","v79","v87","v88","v89","v90","v91","v92","v93","v94","v95","v96","v97","v98","v99","v100","v101","v102","v103","v104","v105","v106","v107","v108","v109","v110","v111","v112","v113","v114","v115","v116","v117","v118","v119","v120","v121","v122","v123","v124","v125","v126","v127","v128","v129","v130","v131","v132","v133","v134","v135","v136","v137","v138","v139","v140","v141","v142","v143","v144","v145","v146","v147","v148","v149","v150","v151","v152","v153","v154","v155","v156","v157","v158","v159","v160","v161","v162","v163","v164","v165","v166","v167","v168","v169","v170","v171","v172","v173","v174","v175","v176","v177","v178","v179","v180","v181","v182","v183","v184","v185","v186","v187","v188","v189","v190","v191"
#define G2_CLOB_A "a0","a1","a2","a3","a4","a5","a6","a7","a8","a9","a10","a11","a12","a13","a14","a15","a16","a17","a18","a19","a20","a21","a22","a23","a24","a25","a26","a27","a28","a29","a30","a31","a32","a33","a34","a35","a36","a37","a38","a39","a40","a41","a42","a43","a44","a45","a46","a47","a48","a49","a50","a51","a52","a53","a54","a55","a56","a57","a58","a59","a60","a61","a62","a63","a64","a65","a66","a67","a68","a69","a70","a71","a72","a73","a74","a75","a76","a77","a78","a79","a80","a81","a82","a83","a84","a85","a86","a87","a88","a89","a90","a91","a92","a93","a94","a95","a96","a97","a98","a99","a100","a101","a102","a103","a104","a105","a106","a107","a108","a109","a110","a111","a112","a113","a114","a115","a116","a117","a118","a119","a120","a121","a122","a123","a124","a125","a126","a127","a128","a129","a130","a131","a132","a133","a134","a135","a136","a137","a138","a139","a140","a141","a142","a143","a144","a145","a146","a147","a148","a149","a150","a151","a152","a153","a154","a155","a156","a157","a158","a159","a160","a161","a162","a163","a164","a165","a166","a167","a168","a169","a170","a171","a172","a173","a174","a175"
#define G2_CLOB_S "vcc","s36","s37","s38","s39","s40","s41","s42","s43","s44","s45","s46","s47","s48","s49","s50","s51","s52","s53","s54","s55","s56","s57","s58","s59","s60","s61","s62","s63","s64","s65","s66","s67","s68","s69","s70","s71","s72","s73","s74","s75","s76","s77","s78","s79","s80","s81","s82","s83","s84","s85","s86","s87","s88","s89","s90","s91","s92","s93","s94","s95","s96","s97"
#define K2MOD 0
#define MODK 1
#define PFMAP 0
#define PFMAP_IN
#define K2_CLOB_V "v0","v1","v2","v3","v4","v5","v6","v7","v8","v9","v10","v11","v12","v13","v14","v15","v16","v17","v18","v19","v20","v21","v22","v23","v24","v25","v26","v27","v28","v29","v30","v31","v32","v33","v34","v35","v36","v37","v38","v39","v40","v41","v42","v43","v44","v45","v46","v47","v48","v49","v50","v51","v52","v53","v54","v55","v56","v57","v58","v59","v60","v61","v62","v63","v64","v65","v66","v67","v68","v69","v70","v71","v72","v73","v74","v75","v76","v77","v78","v79","v80","v81","v82","v83","v84","v85","v86","v87","v88","v89","v90","v91","v92","v93","v94","v95","v109","v110","v111","v112","v113","v114","v115","v116","v117","v118","v119","v120","v121","v122","v123","v124","v125","v126","v127","v128","v129","v130","v131","v132","v133","v134","v135","v136","v137","v138","v139","v140","v141","v142","v143","v144","v146","v147","v148"
#define K2_CLOB_A "a0","a1","a2","a3","a4","a5","a6","a7","a8","a9","a10","a11","a12","a13","a14","a15","a16","a17","a18","a19","a20","a21","a22","a23","a24","a25","a26","a27","a28","a29","a30","a31","a32","a33","a34","a35","a36","a37","a38","a39","a40","a41","a42","a43","a44","a45","a46","a47","a48","a49","a50","a51","a52","a53","a54","a55","a56","a57","a58","a59","a60","a61","a62","a63","a64","a65","a66","a67","a68","a69","a70","a71","a72","a73","a74","a75","a76","a77","a78","a79","a80","a81","a82","a83","a84","a85","a86","a87","a88","a89","a90","a91","a92","a93","a94","a95","a96","a97","a98","a99","a100","a101","a102","a103","a104","a105","a106","a107","a108","a109","a110","a111","a112","a113","a114","a115","a116","a117","a118","a119","a120","a121","a122","a123","a124","a125","a126","a127","a128","a129","a130","a131","a132","a133","a134","a135","a136","a137","a138","a139","a140","a141","a142","a143","a144","a145","a146","a147","a148","a149","a150","a151","a152","a153","a154","a155","a156","a157","a158","a159","a160","a161","a162","a163","a164","a165","a166","a167","a168","a169","a170","a171","a172","a173","a174","a175","a176","a177","a178","a179","a180","a181","a182","a183","a184","a185","a186","a187","a188","a189","a190","a191","a192","a193","a194","a195","a196","a197","a198","a199","a200","a201","a202","a203","a204","a205","a206","a207","a208","a209","a210","a211","a212","a213","a214","a215","a216","a217","a218","a219","a220","a221","a222","a223","a224","a225","a226","a227","a228","a229","a230","a231","a232","a233","a234","a235","a236","a237","a238","a239","a240","a241","a242","a243","a244","a245","a246","a247","a248","a249","a250","a251","a252","a253","a254","a255"
#define K2_CLOB_S "s36","s37","s38","s39","s40","s41","s42","s43","s44","s45","s46","s47","s48","s49","s50","s51","s52","s53","s54","s55","s56","s57","s58","s59","s60","s61","s62","s63","s64","s65","s66","s67","s68","s69","s70","s71","s72","s73","s74","s75","s76","s77","s78","s79","s80","s81","s82","s83","s84","s85","s86","s87","s88","s89","s90","s91","s92","s93"
#define STAMP_ARG
#define STAMP_DECL
#define STAMP_OUTS
#define STAMP_STORE
#define TS_ARG
#define TS_BEGIN
#define TS_END
#define TS_P(i)
#define TS_MID0
#define TS_MID1
#define TS_END2
#define TS_MIDA
#define TS_MIDB
#define TS_DECLB
#define TS_F0
#define TS_F1
#define TS_FEND
#define TS_K2_DECL
#define TS_K2_MOV
#define TS_K2_OUT
#define TS_K2_STORE
#define LD_IN(p) (*(p))
#define LD_WM(p) (*(p))
#define LD_Q(p) (*(p))
#define ST_SC1(p,v) { const u32x4 v_=(v); asm volatile("global_store_dwordx4 %0, %1, off sc1\n s_nop 1"::"v"(p),"v"(v_):"memory"); }
#define ST_DEF(p,v) { *(p)=(v); }
#define ST_KV(p,v) ST_SC1(p,v)
#define ST_Y(p,v) ST_SC1(p,v)
#define ST_X1(p,v) ST_SC1(p,v)
#define ST_H2(p,v) ST_SC1(p,v)
#define PRE_GRID 256+192
#define MID_PF_ISSUE
#define MID_PF_USE
#define MID_PF_LATE
#define MID_PF_LATE2
#define MID_MDWAIT
#define TLB_ARGS
#define PRE_TLB
#define PRE_TLB_USE
#define CB_LABEL(x) asm volatile(x ":");
#define CB_END ".Lcb_end:\n"
#define CODE_PTR(p_,n_,lab) const char* p_; unsigned n_; { unsigned long long pc_; unsigned d_; asm volatile("s_getpc_b64 %0\n.Lp%=:\n s_mov_b32 %1, " lab "-.Lp%=\n s_mov_b32 %2, .Lcb_end-" lab : "=s"(pc_), "=s"(d_), "=s"(n_)); p_=(const char*)(pc_+(long long)(int)d_); }
#define PRE_CPF unsigned cp_=0; if(b<16){ asm volatile("":::"memory"); CODE_PTR(cb_,cn_,".Lcb_pre") cp_=*(const __attribute__((address_space(1))) unsigned*)(cb_+min(t*128u+(b>>3)*32768u,cn_-4u)); asm volatile("":::"memory"); }
#define PRE_CPF_USE asm volatile(""::"v"(cp_));
#define MID_CPF
#define MID_CPF_USE
#define K2_CPF const u16* p3_=pf3; const unsigned p3n_=0x400000u, o3_=ch*4096u;
#define G1_CPF_SETUP "s_getpc_b64 s[84:85]\n.Lq%=:\n s_add_u32 s84, s84, (.Lcb_g1epi-.Lq%=)&4294967295\n s_addc_u32 s85, s85, 0\n s_mov_b32 s86, .Lcb_end-.Lcb_g1epi\n s_mov_b32 s87, 0x20000\n s_lshl_b32 s88, %[gw], 13\n s_and_b32 s88, s88, 0x6000\n v_lshlrev_b32 v249, 3, v246\n"
#define G1_PFW_DECL const unsigned gw_=bid*4+w; const u16* pww_=(gw_<512)?wg:wu; const unsigned pwo_=((7*gw_)%3584u)*128u+8u*458752u;
#define G1_PFW_IN ,[pw]"s"(pww_),[po]"s"(pwo_)
#define G1_PFW_SETUP "s_mov_b64 s[60:61], %[pw]\n s_mov_b32 s62, 0x700000\n s_mov_b32 s63, 0x20000\n s_mov_b32 s79, %[po]\n"
#define MPF_ISSUE
#define MPF_USE
#define MODMAP(jj) (((jj)&~15u)|(((jj)&7u)<<1)|(((jj)>>3)&1u))
#define MID_NT 256
#define MID_MDWAIT2
typedef unsigned short u16;
typedef unsigned u32x2 __attribute__((ext_vector_type(2)));
typedef unsigned u32x4 __attribute__((ext_vector_type(4)));
typedef float f32x2 __attribute__((ext_vector_type(2)));
typedef __bf16 bf16x2 __attribute__((ext_vector_type(2)));
__device__ __forceinline__ float blo(unsigned x){ return __uint_as_float(x<<16); }
__device__ __forceinline__ float bhi(unsigned x){ return __uint_as_float(x&0xffff0000u); }
__device__ __forceinline__ unsigned pk2(float a, float b){ f32x2 v={a,b}; bf16x2 r=__builtin_convertvector(v,bf16x2); return __builtin_bit_cast(unsigned,r); }
__device__ __forceinline__ float rbf(float a){ return blo(pk2(a,a)); }

__device__ __forceinline__ f32x2 up2(unsigned x){ return f32x2{__uint_as_float(x<<16),__uint_as_float(x&0xffff0000u)}; }
__device__ __forceinline__ float dpp_ror(float x, const int n4){ return __builtin_bit_cast(float, n4==4 ? __builtin_amdgcn_update_dpp(0,__builtin_bit_cast(int,x),0x124,0xf,0xf,true) : __builtin_amdgcn_update_dpp(0,__builtin_bit_cast(int,x),0x128,0xf,0xf,true)); }
__device__ __forceinline__ float red2(float a, float b){
  a+=dpp_ror(a,4); b+=dpp_ror(b,4); a+=dpp_ror(a,8); b+=dpp_ror(b,8);
  asm("s_nop 1\n\tv_permlane16_swap_b32_e32 %0, %1" : "+v"(a), "+v"(b));
  float c=a+b, d;
  asm("v_mov_b32 %1, %0\n\ts_nop 1\n\tv_permlane32_swap_b32_e32 %0, %1" : "+v"(c), "=&v"(d));
  return c+d;
}
#define ST_SC1B(base,off,imm,v) { const u32x4 v_=(v); asm volatile("global_store_dwordx4 %0, %1, %2 offset:" #imm " sc1\n s_nop 1"::"v"(off),"v"(v_),"s"(base):"memory"); }

#define G1_LANE (threadIdx.x&63)
#define FUSE_MID 1
#define MLP_EXTRA_ARGS , const u16* __restrict__ q_, const u16* __restrict__ Y, const u16* __restrict__ modp
#define MLP_H2Q
#define MLP_BAR0 "s_barrier\n"
#define MLP_WPF
#define MLP_WPF_USE
__global__ __launch_bounds__(256,1) void k_front(const u16* __restrict__ k, const u16* __restrict__ v, const u16* __restrict__ sk, u16* kvT,
                                                 const u16* __restrict__ c, const u16* __restrict__ wmod, u16* __restrict__ modp, float* __restrict__ gbf, unsigned* flags,
                                                 const u16* __restrict__ hk, const u16* __restrict__ Dv, u16* __restrict__ Y, const u16* pf0, const u16* pf1, const u16* pf2 TS_ARG){
  __shared__ __attribute__((aligned(1024))) char smem[4*13056+4*5120+1024];
  const unsigned b=blockIdx.x, t=threadIdx.x;
  CB_LABEL(".Lcb_pre")
  TS_BEGIN
  const unsigned w=__builtin_amdgcn_readfirstlane(t>>6), lane=t&63;
  const unsigned rb=b>>3, sl=b&7, cg=sl*32+rb, ch=4*cg+w;
  unsigned long long* fl=(unsigned long long*)(flags+FL_BASE+sl*FL_SSTR);
  char* sm=smem+4*13056;
  const unsigned cq=t&31, g=t>>5;
  const int s0=64*rb+8*g; const unsigned c0=128*sl+4*cq;
  u32x2 kk[11], vv[8];
  #pragma unroll
  for(int i=0;i<11;i++){ const int row=s0-3+i; const unsigned rr=(i<3&&row<0)?0u:(unsigned)row; kk[i]=LD_IN((const u32x2*)((const char*)k+(rr*2048u+c0*2u))); }
  #pragma unroll
  for(int i=0;i<8;i++) vv[i]=LD_IN((const u32x2*)((const char*)v+((unsigned)(s0+i)*2048u+c0*2u)));
  const u32x4 w0=*(const u32x4*)((const char*)sk+c0*8u), w1=*(const u32x4*)((const char*)sk+(c0*8u+16u));
  asm volatile("":::"memory");
  unsigned long long tag=__hip_atomic_load(fl+FL_EOFF,__ATOMIC_RELAXED,__HIP_MEMORY_SCOPE_AGENT);
  asm volatile("":::"memory");
  u32x2 ha[8]; unsigned hb[8];
  { const char* hp=(const char*)hk+ch*4096u;
    #pragma unroll
    for(int i=0;i<8;i++){ ha[i]=*(const u32x2*)(hp+lane*8+i*512); hb[i]=*(const unsigned*)((const char*)hk+min(ch*4096u+lane*8+i*512+8,0x3ffffcu)); } }
  const unsigned Dr=Dv[ch+(lane&0)];
  const u32x4 cv0=*(const u32x4*)(c+256*w+16*(lane>>2)), cv1=*(const u32x4*)(c+256*w+16*(lane>>2)+8);
  asm volatile("":::"memory");
  PRE_CPF
  if(s0==0){ kk[0]=u32x2{0,0}; kk[1]=u32x2{0,0}; kk[2]=u32x2{0,0}; }
  #pragma unroll
  for(int cc=0;cc<4;cc++){
    const unsigned wa=(cc<2)?w0[2*cc]:w1[2*cc-4], wb=(cc<2)?w0[2*cc+1]:w1[2*cc-3];
    const float t0=blo(wa), t1=bhi(wa), t2=blo(wb), t3=bhi(wb);
    float kf[11];
    #pragma unroll
    for(int i=0;i<11;i++) kf[i]=(cc&1)?bhi(kk[i][cc>>1]):blo(kk[i][cc>>1]);
    float o[8];
    #pragma unroll
    for(int i=0;i<8;i++){ float ks=kf[i]*t3; ks=fmaf(kf[i+1],t2,ks); ks=fmaf(kf[i+2],t1,ks); ks=fmaf(kf[i+3],t0,ks);
      float vf_=(cc&1)?bhi(vv[i][cc>>1]):blo(vv[i][cc>>1]); o[i]=ks*vf_; }
    u32x4 p; p[0]=pk2(o[7],o[6]); p[1]=pk2(o[5],o[4]); p[2]=pk2(o[3],o[2]); p[3]=pk2(o[1],o[0]);
    *(u32x4*)(sm+(4*cq+cc)*128+((g^(cq&7))*16))=p;
  }
  __syncthreads();
  asm volatile(""::"v"(ha[0]),"v"(ha[1]),"v"(ha[2]),"v"(ha[3]),"v"(ha[4]),"v"(ha[5]),"v"(ha[6]),"v"(ha[7]),"v"(hb[0]),"v"(hb[1]),"v"(hb[2]),"v"(hb[3]),"v"(hb[4]),"v"(hb[5]),"v"(hb[6]),"v"(hb[7]),"v"(cv0),"v"(cv1),"v"(Dr),"v"(tag));
  #pragma unroll
  for(int i=0;i<4;i++){ const unsigned cc=(t>>3)+32*i, gg=t&7;
    u32x4 p=*(const u32x4*)(sm+cc*128+((gg^((cc>>2)&7))*16));
    ST_SC1B(kvT,((128*sl+cc)*2048+64*rb+8*gg)*2,0,p); }
  const unsigned lds0=(unsigned)(size_t)smem;
  const unsigned Ub=lds0+w*13056, Qb=Ub+4608, Tb=lds0+4*13056+w*5120;
  unsigned sv[8];
  #pragma unroll
  for(int e=0;e<8;e++){ const unsigned x=e<4?cv0[e]:cv1[e-4]; const float a0=blo(x), a1=bhi(x); sv[e]=pk2(a0*__builtin_amdgcn_rcpf(1.f+__expf(-a0)),a1*__builtin_amdgcn_rcpf(1.f+__expf(-a1))); }
  const unsigned q=lane>>4, a=lane&15;
  unsigned vf[8];
  for(int p=0;p<8;p++) vf[p]=Qb+256+4*(9+a-8*q)+1024*p;
  unsigned vb=Ub+32+32*a+16*q, vu=Ub+512+2*(16*a+8*(q>>1))+((q&1)?0:8), vt=Tb+40*a+8*q;
  unsigned vl16=lane*16, vl8=lane*8, vuw=Ub+512+lane*16;
  unsigned vmd=((lane&3)==3)?0x80000000u:((256*w+16*(lane>>2))*12288u+(lane&3)*16);
  const u16* pw_=wmod+24*cg; const unsigned pwn_=1024u*12288u-48u*cg;
  const unsigned sD=__builtin_amdgcn_readfirstlane(Dr<<16);
  tag+=0x9E3779B97F4A7C15ull;
  unsigned fo=min(lane,31u)*FL_PSTR;
  asm volatile("":"+v"(sv[0]),"+v"(sv[1]),"+v"(sv[2]),"+v"(sv[3]),"+v"(sv[4]),"+v"(sv[5]),"+v"(sv[6]),"+v"(sv[7]),"+v"(tag),"+v"(fo));
  asm volatile("":"+{v96}"(vf[0]),"+{v97}"(vf[1]),"+{v98}"(vf[2]),"+{v99}"(vf[3]),"+{v100}"(vf[4]),"+{v101}"(vf[5]),"+{v102}"(vf[6]),"+{v103}"(vf[7]),
      "+{v104}"(vb),"+{v105}"(vu),"+{v106}"(vt),"+{v107}"(vl16),"+{v108}"(vl8),"+{v145}"(vuw),"+{v149}"(vmd));
  {
    const unsigned vqw=Qb+lane*4, vpad=Ub+lane*8, vq16=Qb+lane*16;
    asm volatile(
      K2Q_ASM
      "s_waitcnt vmcnt(0)\n"
      : "+{v[112:113]}"(ha[0]),"+{v[116:117]}"(ha[1]),"+{v[120:121]}"(ha[2]),"+{v[124:125]}"(ha[3]),"+{v[128:129]}"(ha[4]),"+{v[132:133]}"(ha[5]),"+{v[136:137]}"(ha[6]),"+{v[140:141]}"(ha[7]),
        "+{v115}"(hb[0]),"+{v119}"(hb[1]),"+{v123}"(hb[2]),"+{v127}"(hb[3]),"+{v131}"(hb[4]),"+{v135}"(hb[5]),"+{v139}"(hb[6]),"+{v143}"(hb[7])
      : "{v109}"(vqw),"{v111}"(vpad),"{v144}"(vq16)
      : "memory","scc","s47","s48","s49","v32","v33","v110","v114","v118","v122","v126","v130","v134","v138","v142");
  }
  PRE_CPF_USE
  TS_F0
  __syncthreads();
  if(t==0) __hip_atomic_store(fl+rb*(FL_PSTR/8),tag,__ATOMIC_RELAXED,__HIP_MEMORY_SCOPE_AGENT);
  if(w==0){
    unsigned it=0;
    if(FL_SLEEP0) __builtin_amdgcn_s_sleep(FL_SLEEP0);
    while(true){ const unsigned long long x=__hip_atomic_load((unsigned long long*)((char*)fl+fo),__ATOMIC_RELAXED,__HIP_MEMORY_SCOPE_AGENT);
      if(__all(x==tag) || ++it>=0x100000u) break;
      __builtin_amdgcn_s_sleep(FL_SLEEP); }
  }
  __builtin_amdgcn_fence(__ATOMIC_ACQUIRE,"wavefront");
  TS_F1
  u32x4 wv[16];
  const unsigned qo_=(32*(4*(b&1)+((b>>3)&3))+8*((b&7)>>1)+(b>>5))*16384u+w*4096u;
  TS_K2_DECL
#define LO(p) ((unsigned)(size_t)(p))
#define HI(p) ((unsigned)((size_t)(p)>>32))
  asm volatile(
    "s_barrier\n"
    "s_mov_b32 s36, %[u0]\n s_mov_b32 s37, %[u1]\n s_mov_b32 s38, 0x400000\n s_mov_b32 s39, 0x20000\n"
    "s_mov_b32 s45, %[ch]\n s_mov_b32 s46, %[dd]\n"
    "s_mov_b64 s[76:77], %[p0]\n s_mov_b32 s78, 0x700000\n s_mov_b32 s79, 0x20000\n s_mov_b64 s[80:81], %[p1]\n s_mov_b32 s82, 0x700000\n s_mov_b32 s83, 0x20000\n"
    "s_mov_b64 s[84:85], %[p2]\n s_mov_b32 s86, " PF2_SIZE "\n s_mov_b32 s53, %[qo]\n s_mov_b32 s87, 0x20000\n s_mov_b64 s[88:89], %[p3]\n s_mov_b32 s90, %[p3n]\n s_mov_b32 s91, 0x20000\n"
    "s_mul_i32 s52, %[chn], " PF_CHUNK "\n"
    K2_ASM
    TS_K2_MOV
    : TS_K2_OUT "={v[160:163]}"(wv[0]),"={v[164:167]}"(wv[1]),"={v[168:171]}"(wv[2]),"={v[172:175]}"(wv[3]),"={v[176:179]}"(wv[4]),"={v[180:183]}"(wv[5]),"={v[184:187]}"(wv[6]),"={v[188:191]}"(wv[7]),
      "={v[192:195]}"(wv[8]),"={v[196:199]}"(wv[9]),"={v[200:203]}"(wv[10]),"={v[204:207]}"(wv[11]),"={v[208:211]}"(wv[12]),"={v[212:215]}"(wv[13]),"={v[216:219]}"(wv[14]),"={v[220:223]}"(wv[15])
    : [u0]"s"(LO(kvT)),[u1]"s"(HI(kvT)),[ch]"s"(ch*4096u),[dd]"s"(sD),[p0]"s"(pf0),[p1]"s"(pf1),[p2]"s"(pf2),[p3]"s"(pw_),[p3n]"s"(pwn_),[chn]"s"(ch),[qo]"s"(qo_),
      "{v96}"(vf[0]),"{v97}"(vf[1]),"{v98}"(vf[2]),"{v99}"(vf[3]),"{v100}"(vf[4]),"{v101}"(vf[5]),"{v102}"(vf[6]),"{v103}"(vf[7]),
      "{v104}"(vb),"{v105}"(vu),"{v106}"(vt),"{v107}"(vl16),"{v108}"(vl8),"{v145}"(vuw),"{v149}"(vmd)
    : "memory","scc",K2_CLOB_S,K2_CLOB_V,K2_CLOB_A);
#undef LO
#undef HI
  TS_K2_STORE
  __syncthreads();
  const char* T=smem+4*13056;
  u32x4 yo[4];
  #pragma unroll
  for(int r=0;r<4;r++){ const unsigned x=256*r+t, row=x>>3, by=40*row+4*(x&7);
    const unsigned c0_=*(const unsigned*)(T+by), c1=*(const unsigned*)(T+5120+by), c2=*(const unsigned*)(T+10240+by), c3=*(const unsigned*)(T+15360+by);
    yo[r][0]=__builtin_amdgcn_perm(c1,c0_,0x05040100u); yo[r][1]=__builtin_amdgcn_perm(c3,c2,0x05040100u); yo[r][2]=__builtin_amdgcn_perm(c1,c0_,0x07060302u); yo[r][3]=__builtin_amdgcn_perm(c3,c2,0x07060302u); }
  #pragma unroll
  for(int r=0;r<4;r++) ST_SC1B(Y,((32*r+(t>>3))*256+cg)*128+(t&7)*16,0,yo[r]);
  f32x2 ac[4];
  #pragma unroll
  for(int e=0;e<4;e++) ac[e]=f32x2{0.f,0.f};
  #pragma unroll
  for(int i=0;i<16;i++){ const float s=(i&1)?bhi(sv[i>>1]):blo(sv[i>>1]); const f32x2 s2={s,s};
    #pragma unroll
    for(int e=0;e<4;e++) ac[e]=__builtin_elementwise_fma(s2,up2(wv[i][e]),ac[e]); }
  float rx[8];
  #pragma unroll
  for(int e=0;e<8;e++){ float x=ac[e>>1][e&1]; x+=dpp_ror(x,4); x+=dpp_ror(x,8); rx[e]=x; }
  asm("s_nop 1\n\tv_permlane16_swap_b32_e32 %0, %1\n\tv_permlane16_swap_b32_e32 %2, %3\n\tv_permlane16_swap_b32_e32 %4, %5\n\tv_permlane16_swap_b32_e32 %6, %7"
      : "+v"(rx[0]),"+v"(rx[1]),"+v"(rx[2]),"+v"(rx[3]),"+v"(rx[4]),"+v"(rx[5]),"+v"(rx[6]),"+v"(rx[7]));
  float y0=rx[0]+rx[1], y1=rx[2]+rx[3], y2=rx[4]+rx[5], y3=rx[6]+rx[7];
  asm("s_nop 1\n\tv_permlane32_swap_b32_e32 %0, %1\n\tv_permlane32_swap_b32_e32 %2, %3" : "+v"(y0),"+v"(y1),"+v"(y2),"+v"(y3));
  float* red=(float*)(smem+4*13056+4*5120);
  if((lane&15)<3){ red[w*32+(lane&3)*8+(lane>>4)]=y0+y1; red[w*32+(lane&3)*8+(lane>>4)+4]=y2+y3; }
  __syncthreads();
  if(t<24){ float x=(red[t]+red[32+t])+(red[64+t]+red[96+t]); unsigned col=24*cg+t, arr=col>>10, d=col&1023;
    unsigned pb=pk2(x,x)&0xffffu;
    modp[arr*1024+((d>>5)*4+((d>>2)&3))*8+((d>>4)&1)*4+(d&3)]=(u16)pb;
    if(arr==5) gbf[d]=__uint_as_float(pb<<16); }
  if(t==64) flags[b*32]=0;
  if(t==128 && b<32) flags[8192+b*64]=0;
  if(t==0 && rb==0) __hip_atomic_store(fl+FL_EOFF,tag,__ATOMIC_RELAXED,__HIP_MEMORY_SCOPE_AGENT);
  TS_FEND
}
#define LO(p) ((unsigned)(size_t)(p))
#define HI(p) ((unsigned)((size_t)(p)>>32))
__global__ __launch_bounds__(256,1) void k_mlp(MLP_H2Q u16* h2t, const u16* __restrict__ wg, const u16* __restrict__ wu, u16* actt, const u16* __restrict__ wd,
                                               MLP_H2Q u16* x1t, const float* __restrict__ gbf, float* xch, unsigned* flags, u16* __restrict__ out MLP_EXTRA_ARGS TS_ARG){
  __shared__ __attribute__((aligned(1024))) char smem[131072];
  const unsigned lds0=(unsigned)(size_t)smem;
  CB_LABEL(".Lcb_mlp")
  TS_BEGIN
  TS_DECLB
  const unsigned bid=blockIdx.x;
  const unsigned w=__builtin_amdgcn_readfirstlane(threadIdx.x>>6);
  {

  constexpr int NI=512/MID_NT, NW=MID_NT/64;
  float (*red)[8][NW]=(float (*)[8][NW])smem;
  const unsigned b=bid, t=threadIdx.x, blk=32*(4*(b&1)+((b>>3)&3))+8*((b&7)>>1)+(b>>5);
  const unsigned j2=t&3, q=(t>>2)&3, m0=t>>4, w=t>>6;
  const unsigned s0=8*blk, TT=s0>>4, jb=(s0&15)+j2;
  constexpr unsigned MS=MID_NT/16;
  u32x2 xa[NI][2][2], ya[NI][2][2]; u32x4 md[5][NI];
  const unsigned oq=((s0+j2)*1024+32*m0+4*q)*2, oy=((TT*256+8*m0+q)*16+jb)*8, om=(m0*4+q)*16;
  #pragma unroll
  for(int i=0;i<NI;i++)
    #pragma unroll
    for(int a=0;a<2;a++)
      #pragma unroll
      for(int nn=0;nn<2;nn++) xa[i][a][nn]=LD_Q((const u32x2*)((const char*)q_+(oq+a*8192+i*MS*64+nn*32)));
  #pragma unroll
  for(int i=0;i<NI;i++)
    #pragma unroll
    for(int a=0;a<2;a++)
      #pragma unroll
      for(int nn=0;nn<2;nn++) ya[i][a][nn]=*(const u32x2*)((const char*)Y+(oy+a*32+i*MS*1024+nn*512));
  #pragma unroll
  for(int ar=0;ar<5;ar++)
    #pragma unroll
    for(int i=0;i<NI;i++) md[ar][i]=*(const u32x4*)((const char*)modp+(om+ar*2048+i*MS*64));
  MID_CPF
  MPF_ISSUE
  f32x2 x[NI][2][4];
  #pragma unroll
  for(int i=0;i<NI;i++)
    #pragma unroll
    for(int a=0;a<2;a++)
      #pragma unroll
      for(int e=0;e<4;e++) x[i][a][e]=up2(xa[i][a][e>>1][e&1]);
  float mean[2], rstd[2];
  f32x2 d[NI][2][4];
  const unsigned tr=j2+((t>>2)&4);
  const bool wr=(t&0x2c)==0;
  auto rd=[&](int slot,int tok){ float r;
    if constexpr(NW==4){ const float4 v=*(const float4*)&red[slot][tok][0]; r=(v.x+v.y)+(v.z+v.w); }
    else { const float4 v=*(const float4*)&red[slot][tok][0], u=*(const float4*)&red[slot][tok][4]; r=((v.x+v.y)+(v.z+v.w))+((u.x+u.y)+(u.z+u.w)); }
    return r; };
  auto stats=[&](int slot){
    float p[2];
    #pragma unroll
    for(int a=0;a<2;a++){ f32x2 s=x[0][a][0]+x[0][a][1]; s+=x[0][a][2]+x[0][a][3];
      #pragma unroll
      for(int i=1;i<NI;i++) s+=(x[i][a][0]+x[i][a][1])+(x[i][a][2]+x[i][a][3]);
      p[a]=s[0]+s[1]; }
    float r=red2(p[0],p[1]);
    if(wr) red[slot][tr][w]=r;
    __syncthreads();
    mean[0]=rd(slot,j2)*(1.f/1024.f); mean[1]=rd(slot,j2+4)*(1.f/1024.f);
    #pragma unroll
    for(int a=0;a<2;a++){ f32x2 s={0.f,0.f};
      #pragma unroll
      for(int i=0;i<NI;i++)
        #pragma unroll
        for(int e=0;e<4;e++){ d[i][a][e]=x[i][a][e]-mean[a]; s=__builtin_elementwise_fma(d[i][a][e],d[i][a][e],s); }
      p[a]=s[0]+s[1]; }
    r=red2(p[0],p[1]);
    if(wr) red[slot+1][tr][w]=r;
    __syncthreads();
    rstd[0]=__builtin_amdgcn_rsqf(rd(slot+1,j2)*(1.f/1024.f)+1e-5f); rstd[1]=__builtin_amdgcn_rsqf(rd(slot+1,j2+4)*(1.f/1024.f)+1e-5f);
  };
  const unsigned ox=((TT*32+m0)*64+q*16+jb)*16, oh=((((TT>>2)*32+m0)*4+(TT&3))*64+q*16+jb)*16;
  stats(0);
  MID_MDWAIT2
  #pragma unroll
  for(int i=0;i<NI;i++){
    f32x2 c1[4], sa[4], ga[4];
    #pragma unroll
    for(int e=0;e<4;e++){ sa[e]=up2(md[0][i][e]); c1[e]=up2(md[1][i][e])+1.f; ga[e]=up2(md[2][i][e]); }
    #pragma unroll
    for(int a=0;a<2;a++){ unsigned pk[4];
      #pragma unroll
      for(int e=0;e<4;e++){
        const f32x2 h=__builtin_elementwise_fma(d[i][a][e],c1[e]*rstd[a],sa[e]);
        const f32x2 r=__builtin_elementwise_fma(ga[e],h*up2(ya[i][a][e>>1][e&1]),x[i][a][e]);
        pk[e]=pk2(r[0],r[1]); x[i][a][e]=up2(pk[e]); }
      const unsigned o=ox+i*MS*1024+a*64;
      ST_SC1B(x1t,o,0,(u32x4{pk[0],pk[1],pk[2],pk[3]})); } }
  stats(2);
  #pragma unroll
  for(int i=0;i<NI;i++){
    f32x2 c1[4], sb[4];
    #pragma unroll
    for(int e=0;e<4;e++){ sb[e]=up2(md[3][i][e]); c1[e]=up2(md[4][i][e])+1.f; }
    #pragma unroll
    for(int a=0;a<2;a++){ unsigned pk[4];
      #pragma unroll
      for(int e=0;e<4;e++){ const f32x2 h=__builtin_elementwise_fma(d[i][a][e],c1[e]*rstd[a],sb[e]); pk[e]=pk2(h[0],h[1]); }
      const unsigned o=oh+i*MS*4096+a*64;
      ST_SC1B(h2t,o,0,(u32x4{pk[0],pk[1],pk[2],pk[3]})); } }
  MPF_USE
  MID_CPF_USE

  }
  asm volatile("s_waitcnt vmcnt(0)":::"memory");
  TS_MIDA
  __syncthreads();
  unsigned* cntA=flags+8192+1024+(4*(bid&1)+((bid>>3)&3))*64;
  unsigned oldA=0;
  if(threadIdx.x==0) oldA=__hip_atomic_fetch_add(cntA,1u,__ATOMIC_RELAXED,__HIP_MEMORY_SCOPE_AGENT);
  {
  const unsigned xcd=bid&7, idx=bid>>3, lane=G1_LANE;
  const unsigned pm=4*(xcd&1)+(idx&3), pn=8*(xcd>>1)+(idx>>2);
  const unsigned odd=pn&1, ii=pn>>1, kap0=7*ii+3*odd;
  const unsigned f0=224*ii+(odd?96:0);
  const u16* wb=((w>>1)?wu:wg)+f0;
  const unsigned wrec=1024u*7168u-f0*2;
  unsigned vgl[4];
  for(int j=0;j<4;j++){ unsigned row=16*(w&1)+4*j+(lane>>4); unsigned sg=row&7; unsigned up=(lane>>1)&7, h=lane&1;
    vgl[j]=row*7168u+((up^sg)*32+h*16)-1024u*j; }
  unsigned vtr[8];
  { unsigned g=lane>>4,qq=(lane>>2)&3,pp=lane&3,sg=qq|((g&1)<<2);
    for(int p=0;p<4;p++) for(int jj=0;jj<2;jj++){ unsigned u=(((p+odd)&3)<<1)|jj; vtr[p*2+jj]=lds0+(4*g+qq)*256+((u^sg)*32)+pp*8; } }
  const unsigned G=4*pm+w;
  unsigned vta=lane*16+(odd?2048:0), vtb=lane*16+(odd?0:2048), vst=lane*16;
  const unsigned sM0=lds0+(w>>1)*8192+(w&1)*4096, sOffT=G*131072u;
  unsigned sst[8];
  for(int p=0;p<4;p++) for(int th=0;th<2;th++){ unsigned tg=2*G+(th^odd), kap=kap0+((p+odd)&3); sst[p*2+th]=(tg*112+kap)*2048u; }
  const u16* pf0=wd;
  G1_PFW_DECL
  if(threadIdx.x==0){
    if(oldA!=31u){ unsigned it=0; while(__hip_atomic_load(cntA,__ATOMIC_RELAXED,__HIP_MEMORY_SCOPE_AGENT)<32u && ++it<0x100000u) __builtin_amdgcn_s_sleep(1); }
  }
  __builtin_amdgcn_fence(__ATOMIC_ACQUIRE,"wavefront");
  TS_MIDB
  asm volatile(
    MLP_BAR0
    "s_mov_b32 s36, %[w0]\n s_mov_b32 s37, %[w1]\n s_mov_b32 s38, %[w2]\n s_mov_b32 s39, 0x20000\n"
    "s_mov_b32 s40, %[t0]\n s_mov_b32 s41, %[t1]\n s_mov_b32 s42, 0x400000\n s_mov_b32 s43, 0x20000\n"
    "s_mov_b32 s44, %[a0]\n s_mov_b32 s45, %[a1]\n s_mov_b32 s46, 0xe00000\n s_mov_b32 s47, 0x20000\n"
    "s_mov_b32 s48, %[m0]\n s_mov_b32 s49, 0\n s_mov_b32 s50, %[ot]\n"
    "s_mov_b32 s52, %[s0]\n s_mov_b32 s53, %[s1]\n s_mov_b32 s54, %[s2]\n s_mov_b32 s55, %[s3]\n"
    "s_mov_b32 s56, %[s4]\n s_mov_b32 s57, %[s5]\n s_mov_b32 s58, %[s6]\n s_mov_b32 s59, %[s7]\n"
    "s_mov_b64 s[80:81], %[p0]\n s_mov_b32 s82, 0x700000\n s_mov_b32 s83, 0x20000\n s_mul_i32 s78, %[gw], 7168\n"
    "v_add_u32 v232, 0x10000, v224\n v_add_u32 v233, 0x10000, v225\n v_add_u32 v234, 0x10000, v226\n v_add_u32 v235, 0x10000, v227\n"
    "v_add_u32 v236, 0x10000, v228\n v_add_u32 v237, 0x10000, v229\n v_add_u32 v238, 0x10000, v230\n v_add_u32 v239, 0x10000, v231\n"
    G1_CPF_SETUP
    G1_PFW_SETUP
    G1_ASM
    "s_barrier\n"
    : "+{v224}"(vtr[0]),"+{v225}"(vtr[1]),"+{v226}"(vtr[2]),"+{v227}"(vtr[3]),"+{v228}"(vtr[4]),"+{v229}"(vtr[5]),"+{v230}"(vtr[6]),"+{v231}"(vtr[7]),
      "+{v240}"(vgl[0]),"+{v241}"(vgl[1]),"+{v242}"(vgl[2]),"+{v243}"(vgl[3]),"+{v244}"(vta),"+{v245}"(vtb),"+{v246}"(vst)
    : [w0]"s"(LO(wb)),[w1]"s"(HI(wb)),[w2]"s"(wrec),[t0]"s"(LO(h2t)),[t1]"s"(HI(h2t)),[a0]"s"(LO(actt)),[a1]"s"(HI(actt)),
      [m0]"s"(sM0),[p0]"s"(pf0),[gw]"s"(bid*4+w) G1_PFW_IN,[ot]"s"(sOffT),[s0]"s"(sst[0]),[s1]"s"(sst[1]),[s2]"s"(sst[2]),[s3]"s"(sst[3]),[s4]"s"(sst[4]),[s5]"s"(sst[5]),[s6]"s"(sst[6]),[s7]"s"(sst[7])
    : "memory","scc",G1_CLOB_S,G1_CLOB_V,G1_CLOB_A);
  }
  unsigned lane, bid2;
  asm volatile("v_mbcnt_lo_u32_b32 %0, -1, 0\n v_mbcnt_hi_u32_b32 %0, -1, %0\n s_mov_b32 %1, %2" : "=&v"(lane), "=&s"(bid2) : "s"(bid));
  TS_MID0
  const unsigned xcd=bid2&7, idx=bid2>>3;
  const unsigned kh=xcd>>2, pm=2*(4*(xcd&1)+(idx&3))+((idx>>2)&1), pn=4*((xcd>>1)&1)+(idx>>3);
  unsigned* cnt=flags+8192+((pm>>1)*2+kh)*64;
  unsigned old=0;
  if(w==0 && lane==0) old=__hip_atomic_fetch_add(cnt,1u,__ATOMIC_RELAXED,__HIP_MEMORY_SCOPE_AGENT);
  const u16* wb=wd+(size_t)kh*1792*1024+pn*128;
  const unsigned wrec=1792u*2048u-pn*256u;
  unsigned vgl[4];
  for(int j=0;j<4;j++){ unsigned row=16*w+4*j+(lane>>4); unsigned sg=row&7; unsigned up=(lane>>1)&7,h=lane&1; vgl[j]=row*2048u+((up^sg)*32+h*16)-1024u*j; }
  unsigned vtr[8];
  { unsigned q=lane>>4,qq=(lane>>2)&3,pp=lane&3; unsigned row=4*q+qq, sg=row&7;
    for(int n=0;n<8;n++){ unsigned u=n^(4*(1-kh)); vtr[n]=lds0+row*256+((u^sg)*32)+pp*8; } }
  const unsigned T=pm*8+pn;
  const unsigned sM0=lds0+w*4096;
  const unsigned sOffT=((4*pm+w)*112+kh*56)*2048u;
  const unsigned sXS=((T*2+kh)*4+w)*8192u, sXR=((T*2+(kh^1))*4+w)*8192u;
  const unsigned sFS=(T*2+kh)*128u, sFR=(T*2+(kh^1))*128u;
  const unsigned sX1=((8*pm+2*w)*32+4*pn+2*kh)*1024u;
  const unsigned sGB=(128*pn+64*kh)*4u;
  const unsigned sO0=((128*pm+32*w)*1024+128*pn+64*kh)*2u, sO1=sO0+16*2048u;
  const unsigned q=lane>>4;
  const unsigned vtk=lane*16, vgb=q*16, vout=(lane&15)*2048u+((q&1)*16+(q>>1)*8)*2;
  MLP_WPF
  if(w==0 && lane==0){
    if(old!=15u){ unsigned it=0; while(__hip_atomic_load(cnt,__ATOMIC_RELAXED,__HIP_MEMORY_SCOPE_AGENT)<16u && ++it<0x100000u) __builtin_amdgcn_s_sleep(1); }
  }
  __builtin_amdgcn_fence(__ATOMIC_ACQUIRE,"wavefront");
  MLP_WPF_USE
  TS_MID1
  asm volatile(
    "s_barrier\n"
    "s_mov_b32 s36, %[w0]\n s_mov_b32 s37, %[w1]\n s_mov_b32 s38, %[w2]\n s_mov_b32 s39, 0x20000\n"
    "s_mov_b32 s40, %[a0]\n s_mov_b32 s41, %[a1]\n s_mov_b32 s42, 0xe00000\n s_mov_b32 s43, 0x20000\n"
    "s_mov_b32 s44, %[x0]\n s_mov_b32 s45, %[x1]\n s_mov_b32 s46, 0x800000\n s_mov_b32 s47, 0x20000\n"
    "s_mov_b32 s48, %[o0]\n s_mov_b32 s49, %[o1]\n s_mov_b32 s50, 0x400000\n s_mov_b32 s51, 0x20000\n"
    "s_mov_b32 s52, %[r0]\n s_mov_b32 s53, %[r1]\n s_mov_b32 s54, 0x400000\n s_mov_b32 s55, 0x20000\n"
    "s_mov_b32 s56, %[m0]\n s_mov_b32 s57, %[m1]\n s_mov_b32 s58, 4096\n s_mov_b32 s59, 0x20000\n"
    "s_mov_b32 s60, %[f0]\n s_mov_b32 s61, %[f1]\n s_mov_b32 s62, 32768\n s_mov_b32 s63, 0x20000\n"
    "s_mov_b32 s64, %[lm]\n s_mov_b32 s65, 0\n s_mov_b32 s66, %[ot]\n"
    "s_mov_b32 s68, %[xs]\n s_mov_b32 s69, %[xr]\n s_mov_b32 s70, %[fs]\n s_mov_b32 s71, %[fr]\n"
    "s_mov_b32 s72, %[r1o]\n s_mov_b32 s73, %[gb]\n s_mov_b32 s74, %[wv]\n s_mov_b32 s96, %[so0]\n s_mov_b32 s97, %[so1]\n"
    "v_add_u32 v72, 0x10000, v64\n v_add_u32 v73, 0x10000, v65\n v_add_u32 v74, 0x10000, v66\n v_add_u32 v75, 0x10000, v67\n"
    "v_add_u32 v76, 0x10000, v68\n v_add_u32 v77, 0x10000, v69\n v_add_u32 v78, 0x10000, v70\n v_add_u32 v79, 0x10000, v71\n"
    G2_ASM
    CB_END
    :
    : [w0]"s"(LO(wb)),[w1]"s"(HI(wb)),[w2]"s"(wrec),[a0]"s"(LO(actt)),[a1]"s"(HI(actt)),[x0]"s"(LO(xch)),[x1]"s"(HI(xch)),
      [o0]"s"(LO(out)),[o1]"s"(HI(out)),[r0]"s"(LO(x1t)),[r1]"s"(HI(x1t)),[m0]"s"(LO(gbf)),[m1]"s"(HI(gbf)),[f0]"s"(LO(flags)),[f1]"s"(HI(flags)),
      [lm]"s"(sM0),[ot]"s"(sOffT),[xs]"s"(sXS),[xr]"s"(sXR),[fs]"s"(sFS),[fr]"s"(sFR),[r1o]"s"(sX1),[gb]"s"(sGB),[wv]"s"(w),[so0]"s"(sO0),[so1]"s"(sO1),
      "{v64}"(vtr[0]),"{v65}"(vtr[1]),"{v66}"(vtr[2]),"{v67}"(vtr[3]),"{v68}"(vtr[4]),"{v69}"(vtr[5]),"{v70}"(vtr[6]),"{v71}"(vtr[7]),
      "{v80}"(vgl[0]),"{v81}"(vgl[1]),"{v82}"(vgl[2]),"{v83}"(vgl[3]),"{v84}"(vtk),"{v85}"(vgb),"{v86}"(vout)
    : "memory","scc",G2_CLOB_S,G2_CLOB_V,G2_CLOB_A);
  TS_END2
}
#undef LO
#undef HI
extern "C" void kernel_launch(void* const* d_in, const int* in_sizes, int n_in, void* d_out, int out_size, void* d_ws, size_t ws_size, hipStream_t stream){
  const u16* q=(const u16*)d_in[0]; const u16* k=(const u16*)d_in[1]; const u16* v=(const u16*)d_in[2];
  const u16* sk=(const u16*)d_in[3]; const u16* hk=(const u16*)d_in[4]; const u16* Dv=(const u16*)d_in[5];
  const u16* wg=(const u16*)d_in[7]; const u16* wu=(const u16*)d_in[8]; const u16* wd=(const u16*)d_in[9];
  const u16* c=(const u16*)d_in[10]; const u16* wmod=(const u16*)d_in[11];
  char* ws=(char*)d_ws;
  u16* kvT=(u16*)ws; u16* Y=(u16*)(ws+(4u<<20)); u16* h2t=(u16*)(ws+(8u<<20)); u16* x1t=(u16*)(ws+(12u<<20)); u16* act=(u16*)(ws+(16u<<20));
  u16* modp=(u16*)(ws+(31u<<20)); float* gbf=(float*)(ws+(31u<<20)+16384); unsigned* flags=(unsigned*)(ws+(31u<<20)+32768);
  u16* sT=(u16*)(ws+(31u<<20)+24576);
  float* part=(float*)(ws+(30u<<20)+(512u<<10));
  float* xch=(float*)(ws+(32u<<20));
  k_front<<<256,256,0,stream>>>(k,v,sk,kvT,c,wmod,modp,gbf,flags,hk,Dv,Y,wg,wu,wd TS_P(0));
  k_mlp<<<256,256,0,stream>>>(h2t,wg,wu,act,wd,x1t,gbf,xch,flags,(u16*)d_out,q,Y,modp TS_P(2));
}
```
